```python
import torch

def _softmax(x, axis=-1):
    return torch.softmax(x, dim=axis)
def _gelu(x):
    return torch.nn.functional.gelu(x, approximate="tanh")
def _silu(x):
    return torch.nn.functional.silu(x)
def _sigmoid(x):
    return torch.sigmoid(x)
def _layernorm(x, axis=-1, eps=1e-5):
    m = x.mean(dim=axis, keepdim=True)
    v = x.var(dim=axis, keepdim=True, unbiased=False)
    return (x - m) / torch.sqrt(v + eps)
def _scatter_add(tgt, idx, src, axis=0):
    return tgt.clone().index_add_(axis, idx, src)
def _segment_sum(src, seg, n):
    out = torch.zeros((n,) + src.shape[1:], dtype=src.dtype)
    return out.index_add_(0, seg, src)
def _pad(x, pw):
    flat = tuple(v for pair in reversed(pw) for v in pair)
    return torch.nn.functional.pad(x, flat)

kernel_name = 'sequential_preLN__sliding_block_cached__dense_activation_relu__decode'

def setup_inputs():

    _w = (lambda a: (lambda t: t.to(torch.bfloat16) if t.is_floating_point() else t)(torch.as_tensor(a)))
    def _A_setup():
        B, Sq, D = 1, 1, 640
        Hq, Hkv, Sk, Dh = 32, 32, 4096, 128
        assert Hq % Hkv == 0, (Hq, Hkv)
        return {
            "x": _w((lambda *s: torch.randn(*s, dtype=torch.bfloat16) * 0.05)(B, Sq, D)),
            "k_cache": _w((lambda *s: torch.randn(*s, dtype=torch.bfloat16) * 3.2)(B, Hkv, Sk, Dh)),
            "v_cache": _w((lambda *s: torch.randn(*s, dtype=torch.bfloat16) * 0.4)(B, Hkv, Sk, Dh)),
            "wq": _w((lambda *s: torch.randn(*s, dtype=torch.bfloat16) * 3.2)(D, Hq * Dh) * (D ** -0.5)),
            "wo": _w((lambda *s: torch.randn(*s, dtype=torch.bfloat16) * 0.4)(Hq * Dh, D) * ((Hq * Dh) ** -0.5)),
        }
    _A_in = _A_setup()

    def _B_setup():
        B, S, D, Dff = 1, 1, 640, 1728
        return {
            "x": _w((lambda *s: torch.randn(*s, dtype=torch.bfloat16) * 0.05)(B, S, D)),
            "w1": _w((lambda *s: torch.randn(*s, dtype=torch.bfloat16) * 0.05)(D, Dff)),
            "w2": _w((lambda *s: torch.randn(*s, dtype=torch.bfloat16) * 0.05)(Dff, D)),
        }
    _B_in = _B_setup()

    return {
        "A_x": _A_in["x"],
        "A_k_cache": _A_in["k_cache"],
        "A_v_cache": _A_in["v_cache"],
        "A_wq": _A_in["wq"],
        "A_wo": _A_in["wo"],
        "B_x": _B_in["x"],
        "B_w1": _B_in["w1"],
        "B_w2": _B_in["w2"],
    }

def reference(A_x, A_k_cache, A_v_cache, A_wq, A_wo, B_x, B_w1, B_w2):
    def _A_ref(x, k_cache, v_cache, wq, wo):
        B, Sq, D = x.shape
        Hq, Dh = 32, 128
        Hkv, Sk = k_cache.shape[1], k_cache.shape[2]
        W = min(4096, max(256, Sk // 4))
        rep = Hq // Hkv
        q = (lambda x, *d: x.permute(*d))((x @ wq).reshape(B, Sq, Hq, Dh), 0, 2, 1, 3)
        k = (lambda x, n, axis: torch.repeat_interleave(x, n, dim=axis))(k_cache[:, :, Sk - W:, :], rep, axis=1)
        v = (lambda x, n, axis: torch.repeat_interleave(x, n, dim=axis))(v_cache[:, :, Sk - W:, :], rep, axis=1)
        scores = torch.einsum("bhqd,bhkd->bhqk", q, k) * (Dh ** -0.5)
        p = _softmax(scores, axis=-1)
        o = torch.einsum("bhqk,bhkd->bhqd", p, v)
        return (lambda x, *d: x.permute(*d))(o, 0, 2, 1, 3).reshape(B, Sq, Hq * Dh) @ wo

    def _B_ref(x, w1, w2):
        h = torch.einsum("bsd,df->bsf", x, w1)
        zero = torch.zeros_like(h)
        h = torch.maximum(h, zero)
        return torch.einsum("bsf,fd->bsd", h, w2)

    x = A_x
    h = x + _A_ref(_layernorm(x, axis=-1), A_k_cache, A_v_cache, A_wq, A_wo)
    h2 = h + _B_ref(_layernorm(h, axis=-1), B_w1, B_w2)
    return h2
```

```cpp
#include <hip/hip_runtime.h>
#include <hip/hip_bf16.h>
#include <stdint.h>

typedef unsigned short u16;
typedef unsigned u32x4 __attribute__((ext_vector_type(4)));

constexpr int D = 640, H = 32, DH = 128, SK = 4096, W = 1024, DFF = 1728;

__device__ __forceinline__ float bf2f(u16 b) { return __uint_as_float(((unsigned)b) << 16); }
__device__ __forceinline__ float rbf(float f) {
  return __bfloat162float(__float2bfloat16(f)); }
__device__ __forceinline__ float lo(unsigned u) { return __uint_as_float(u << 16); }
__device__ __forceinline__ float hi(unsigned u) { return __uint_as_float(u & 0xffff0000u); }

__device__ float block_sum(float v, float* red) {
  for (int o = 32; o > 0; o >>= 1) v += __shfl_xor(v, o);
  int w = threadIdx.x >> 6, nw = blockDim.x >> 6;
  __syncthreads();
  if ((threadIdx.x & 63) == 0) red[w] = v;
  __syncthreads();
  float s = 0; for (int i = 0; i < nw; ++i) s += red[i];
  return s;
}
__device__ void layernorm(const float* xin, float* xn, float* red) {
  float s = 0; for (int i = threadIdx.x; i < D; i += blockDim.x) s += xin[i];
  float m = block_sum(s, red) / D;
  float v = 0; for (int i = threadIdx.x; i < D; i += blockDim.x) { float d = xin[i] - m; v += d * d; }
  float var = block_sum(v, red) / D;
  float r = 1.0f / sqrtf(var + 1e-5f);
  for (int i = threadIdx.x; i < D; i += blockDim.x) xn[i] = rbf((xin[i] - m) * r);
  __syncthreads();
}

__global__ void __launch_bounds__(256) k1(const u16* x, const u16* kc, const u16* vc, const u16* wq, float* part) {
  __shared__ float xs[D], xn[D], red[8], q[DH], sc[128], qp[16][DH];
  int b = blockIdx.x; int h = (b & 7) * 4 + ((b >> 3) & 3), s = b >> 5;
  int t = threadIdx.x;
  for (int i = t; i < D; i += 256) xs[i] = bf2f(x[i]);
  __syncthreads();
  layernorm(xs, xn, red);
  { int c = t & 15, r = t >> 4; float acc[8] = {0};
    for (int i = r; i < D; i += 16) { u32x4 w = *(const u32x4*)(wq + (size_t)i * 4096 + h * DH + c * 8); float xv = xn[i];
      for (int k = 0; k < 4; ++k) { acc[2*k] += xv * lo(w[k]); acc[2*k+1] += xv * hi(w[k]); } }
    for (int k = 0; k < 8; ++k) qp[r][c * 8 + k] = acc[k];
    __syncthreads();
    if (t < DH) { float a = 0; for (int r2 = 0; r2 < 16; ++r2) a += qp[r2][t]; q[t] = rbf(a); }
    __syncthreads(); }
  const u16* kb = kc + ((size_t)h * SK + (SK - W) + s * 128) * DH;
  const u16* vb = vc + ((size_t)h * SK + (SK - W) + s * 128) * DH;
  { int c = t & 15, r = t >> 4;
    for (int key = r; key < 128; key += 16) { u32x4 w = *(const u32x4*)(kb + key * DH + c * 8); float a = 0;
      for (int k = 0; k < 4; ++k) a += q[c*8+2*k] * lo(w[k]) + q[c*8+2*k+1] * hi(w[k]);
      for (int o = 8; o > 0; o >>= 1) a += __shfl_xor(a, o);
      if (c == 0) sc[key] = a * 0.08838834764831845f; }
    __syncthreads(); }
  float m = -INFINITY; for (int i = 0; i < 128; ++i) m = fmaxf(m, sc[i]);
  __syncthreads();
  if (t < 128) sc[t] = __expf(sc[t] - m);
  __syncthreads();
  float l = 0; for (int i = 0; i < 128; ++i) l += sc[i];
  { int c = t & 15, r = t >> 4; float acc[8] = {0};
    for (int key = r; key < 128; key += 16) { u32x4 w = *(const u32x4*)(vb + key * DH + c * 8); float p = sc[key];
      for (int k = 0; k < 4; ++k) { acc[2*k] += p * lo(w[k]); acc[2*k+1] += p * hi(w[k]); } }
    for (int k = 0; k < 8; ++k) qp[r][c * 8 + k] = acc[k];
    __syncthreads();
    float* o = part + (h * 8 + s) * 132;
    if (t < DH) { float a = 0; for (int r2 = 0; r2 < 16; ++r2) a += qp[r2][t]; o[t] = a; }
    if (t == 0) { o[128] = m; o[129] = l; } }
}
__global__ void __launch_bounds__(128) k2(const float* part, const u16* wo, float* po) {
  __shared__ float o[16];
  int h = blockIdx.x >> 3, s = blockIdx.x & 7, t = threadIdx.x;
  if (t < 16) { float M = -INFINITY; for (int i = 0; i < 8; ++i) M = fmaxf(M, part[(h*8+i)*132+128]);
    float L = 0, a = 0; for (int i = 0; i < 8; ++i) { float w = __expf(part[(h*8+i)*132+128] - M); L += w * part[(h*8+i)*132+129]; a += w * part[(h*8+i)*132 + s*16 + t]; }
    o[t] = rbf(a / L); }
  __syncthreads();
  if (t < 80) { float acc[8] = {0};
    for (int r = 0; r < 16; ++r) { u32x4 w = *(const u32x4*)(wo + (size_t)(h * 128 + s * 16 + r) * D + t * 8); float p = o[r];
      for (int k = 0; k < 4; ++k) { acc[2*k] += p * lo(w[k]); acc[2*k+1] += p * hi(w[k]); } }
    for (int k = 0; k < 8; ++k) po[blockIdx.x * D + t * 8 + k] = acc[k]; }
}
__global__ void __launch_bounds__(256) k3(const u16* x, const float* po, float* hb) {
  __shared__ float r[16][17];
  int c = threadIdx.x & 15, g = threadIdx.x >> 4, col = blockIdx.x * 16 + c;
  float a = 0; for (int i = g; i < 256; i += 16) a += po[i * D + col];
  r[g][c] = a; __syncthreads();
  if (threadIdx.x < 16) { float s = 0; for (int i = 0; i < 16; ++i) s += r[i][c]; hb[col] = rbf(bf2f(x[col]) + rbf(s)); }
}
__global__ void __launch_bounds__(256) k4(const float* hb, const u16* w1, const u16* w2, float* p2) {
  __shared__ float xs[D], xn[D], red[8], pr[256][9], h1[8];
  int t = threadIdx.x, b = blockIdx.x;
  for (int i = t; i < D; i += 256) xs[i] = hb[i];
  __syncthreads();
  layernorm(xs, xn, red);
  float acc[8] = {0};
  for (int i = t; i < D; i += 256) { u32x4 w = *(const u32x4*)(w1 + (size_t)i * DFF + b * 8); float xv = xn[i];
    for (int k = 0; k < 4; ++k) { acc[2*k] += xv * lo(w[k]); acc[2*k+1] += xv * hi(w[k]); } }
  for (int k = 0; k < 8; ++k) pr[t][k] = acc[k];
  __syncthreads();
  if (t < 8) { float s = 0; for (int i = 0; i < 256; ++i) s += pr[i][t]; h1[t] = fmaxf(rbf(s), 0.f); }
  __syncthreads();
  if (t < 80) { float a2[8] = {0};
    for (int r = 0; r < 8; ++r) { u32x4 w = *(const u32x4*)(w2 + (size_t)(b * 8 + r) * D + t * 8); float p = h1[r];
      for (int k = 0; k < 4; ++k) { a2[2*k] += p * lo(w[k]); a2[2*k+1] += p * hi(w[k]); } }
    for (int k = 0; k < 8; ++k) p2[b * D + t * 8 + k] = a2[k]; }
}
__global__ void __launch_bounds__(256) k5(const float* hb, const float* p2, __hip_bfloat16* out) {
  __shared__ float r[16][17];
  int c = threadIdx.x & 15, g = threadIdx.x >> 4, col = blockIdx.x * 16 + c;
  float a = 0; for (int i = g; i < 216; i += 16) a += p2[i * D + col];
  r[g][c] = a; __syncthreads();
  if (threadIdx.x < 16) { float s = 0; for (int i = 0; i < 16; ++i) s += r[i][c]; out[col] = __float2bfloat16(hb[col] + rbf(s)); }
}

extern "C" void kernel_launch(void* const* d_in, const int* in_sizes, int n_in,
                              void* d_out, int out_size, void* d_ws, size_t ws_size,
                              hipStream_t stream) {
  const u16* x = (const u16*)d_in[0]; const u16* kc = (const u16*)d_in[1]; const u16* vc = (const u16*)d_in[2];
  const u16* wq = (const u16*)d_in[3]; const u16* wo = (const u16*)d_in[4];
  const u16* w1 = (const u16*)d_in[6]; const u16* w2 = (const u16*)d_in[7];
  float* ws = (float*)d_ws;
  float* part = ws; float* po = ws + 65536; float* hb = po + 256 * D; float* p2 = hb + 1024;
  k1<<<256, 256, 0, stream>>>(x, kc, vc, wq, part);
  k2<<<256, 128, 0, stream>>>(part, wo, po);
  k3<<<40, 256, 0, stream>>>(x, po, hb);
  k4<<<216, 256, 0, stream>>>(hb, w1, w2, p2);
  k5<<<40, 256, 0, stream>>>(hb, p2, (__hip_bfloat16*)d_out);
}
```

```cpp
#include <hip/hip_runtime.h>
#include <hip/hip_bf16.h>
#include <stdint.h>

typedef unsigned u32;
typedef unsigned long long u64;
typedef u32 u32x2 __attribute__((ext_vector_type(2)));
typedef u32 u32x4 __attribute__((ext_vector_type(4)));
typedef float f32x2 __attribute__((ext_vector_type(2)));
typedef float f32x4 __attribute__((ext_vector_type(4)));
typedef __bf16 bf16x2 __attribute__((ext_vector_type(2)));
typedef __bf16 bf16x8 __attribute__((ext_vector_type(8)));
typedef short s16x4 __attribute__((ext_vector_type(4)));
typedef __amdgpu_buffer_rsrc_t rsrc_t;
#define LAS __attribute__((address_space(3)))
#define DEV __device__ __forceinline__

constexpr int D = 640, SK = 4096, WIN = 1024, DFF = 1728;
constexpr u32 WS_QP = 4096;
constexpr u32 WS_OP = WS_QP + 32 * 8 * 1024;
constexpr u32 WS_ML = WS_OP + 32 * 8 * 1024;
constexpr u32 WS_Y = WS_ML + 32 * 8 * 16;
constexpr u32 WS_P4 = WS_Y + 8 * 640 * 8;
constexpr u32 WS_END = WS_P4 + 54 * 640 * 8;
constexpr u32 WS_ST = 16u << 20;
constexpr int NT = 2, SC1 = 16;
constexpr u32 SPIN_CAP = 1u << 18;
constexpr u32 L_RED = 0, L_QS = 4096, L_ML = L_QS + 256, L_HS = L_ML + 64, L_STAT = L_HS + 1280, L_ZP = L_STAT + 64,
              L_W1 = 8192, L_W2 = L_W1 + 40960, L_END = L_W2 + 40960;

#define EX0 2
#define EX1 2
#define EX2 2

DEV rsrc_t mk(const void* p, u32 bytes) { return __builtin_amdgcn_make_buffer_rsrc((void*)p, (short)0, (int)bytes, 0x00020000); }
template <int AUX> DEV u32x4 ld16(rsrc_t r, u32 v, u32 s) { return __builtin_amdgcn_raw_buffer_load_b128(r, v, s, AUX); }
template <int AUX> DEV u32 ld4(rsrc_t r, u32 v, u32 s) { return __builtin_amdgcn_raw_buffer_load_b32(r, v, s, AUX); }
DEV float f_(u32 u) { return __builtin_bit_cast(float, u); }
DEV u32 u_(float f) { return __builtin_bit_cast(u32, f); }
DEV float blo(u32 u) { return f_(u << 16); }
DEV float bhi(u32 u) { return f_(u & 0xffff0000u); }
DEV u32 pk(float lo, float hi) { f32x2 v = {lo, hi}; return __builtin_bit_cast(u32, __builtin_convertvector(v, bf16x2)); }
DEV float rbf(float f) { return blo(pk(f, f)); }
template <int C> DEV float dpp(float x) { return f_(__builtin_amdgcn_mov_dpp(u_(x), C, 0xf, 0xf, true)); }
template <int C> DEV u32 dppu(u32 x) { return __builtin_amdgcn_mov_dpp(x, C, 0xf, 0xf, true); }
constexpr int XOR1 = 0xB1, XOR2 = 0x4E, HMIR = 0x141, ROR8 = 0x128, BC = 0x150;
DEV float sw16_add(float x) { auto s = __builtin_amdgcn_permlane16_swap(u_(x), u_(x), false, false); return f_(s[0]) + f_(s[1]); }
DEV float sw32_add(float x) { auto s = __builtin_amdgcn_permlane32_swap(u_(x), u_(x), false, false); return f_(s[0]) + f_(s[1]); }
DEV float sw16_max(float x) { auto s = __builtin_amdgcn_permlane16_swap(u_(x), u_(x), false, false); return fmaxf(f_(s[0]), f_(s[1])); }
DEV float sw32_max(float x) { auto s = __builtin_amdgcn_permlane32_swap(u_(x), u_(x), false, false); return fmaxf(f_(s[0]), f_(s[1])); }
DEV float oct_add(float x) { x += dpp<XOR1>(x); x += dpp<XOR2>(x); return x + dpp<HMIR>(x); }
DEV float oct_max(float x) { x = fmaxf(x, dpp<XOR1>(x)); x = fmaxf(x, dpp<XOR2>(x)); return fmaxf(x, dpp<HMIR>(x)); }
DEV float xoct_add(float x) { x += dpp<ROR8>(x); return sw32_add(sw16_add(x)); }
DEV float wave_add(float x) { return xoct_add(oct_add(x)); }
DEV float dot2(u32 a, u32 b, float c) { return __builtin_amdgcn_fdot2_f32_bf16(__builtin_bit_cast(bf16x2, a), __builtin_bit_cast(bf16x2, b), c, false); }
DEV f32x4 mfma(u32x4 a, u32x4 b, f32x4 c) { return __builtin_amdgcn_mfma_f32_16x16x32_bf16(__builtin_bit_cast(bf16x8, a), __builtin_bit_cast(bf16x8, b), c, 0, 0, 0); }
DEV u32x4 diag(u32 dup, u32x4 m) { return u32x4{dup & m[0], dup & m[1], dup & m[2], dup & m[3]}; }
#define SB() __builtin_amdgcn_sched_barrier(0)
#define ARRIVED(v) do { asm volatile("" :: "v"(v)); SB(); } while (0)
DEV void wg_barrier() { asm volatile("s_waitcnt lgkmcnt(0)\n\ts_barrier" ::: "memory"); }
DEV u32x2 trrd(LAS char* p) { return __builtin_bit_cast(u32x2, __builtin_amdgcn_ds_read_tr16_b64_v4i16((LAS s16x4*)p)); }
DEV void ln_stats(float s1, float s2, float& mean_b, float& rs) {
  const float mean = s1 * (1.0f / D);
  const float var = fmaxf(s2 * (1.0f / D) - mean * mean, 0.0f);
  mean_b = rbf(mean);
  rs = __builtin_amdgcn_rcpf(rbf(__builtin_amdgcn_sqrtf(rbf(rbf(var) + 1e-5f))));
}
#define STAMP(i)
#define FLUSH_STAMPS()

__global__ void __launch_bounds__(512, 2)
fused_block(const void* __restrict__ x, const void* __restrict__ kc, const void* __restrict__ vc, const void* __restrict__ wq,
            const void* __restrict__ wo, const void* __restrict__ w1, const void* __restrict__ w2, void* __restrict__ out, void* ws) {
  __shared__ __attribute__((aligned(16))) char lds_[L_END];
  LAS char* lds = (LAS char*)lds_;
  const u32 b = blockIdx.x, xcd = b & 7, j = b >> 3, h = xcd * 4 + (j & 3), s = j >> 2;
  const u32 w = __builtin_amdgcn_readfirstlane(threadIdx.x >> 6), l = threadIdx.x & 63, n = l & 15, kg = l >> 4;
  STAMP(0);
  asm volatile("" :: "s"(x), "s"(kc), "s"(vc), "s"(wq), "s"(wo), "s"(w1), "s"(w2), "s"(out), "s"(ws));
  const rsrc_t rX = mk(x, D * 2), rQ = mk(wq, D * 4096 * 2), rWS = mk(ws, WS_END);
  const u32 kvbase = (h * SK + (SK - WIN) + s * 128 + ((w + j) & 7) * 16) * 256;
  const rsrc_t rK = mk(kc, 32u * SK * 256), rV = mk(vc, 32u * SK * 256);
  const u32 qrow = s * 80 + w * 10;
  u32x4 Q[3], K[4], V[4], ex[10];
  Q[0] = ld16<NT>(rQ, (2 * kg) * 8192 + n * 16, qrow * 8192 + h * 256);
  Q[1] = ld16<NT>(rQ, (2 * kg + 1) * 8192 + n * 16, qrow * 8192 + h * 256);
  Q[2] = ld16<NT>(rQ, (8 + (kg & 1)) * 8192 + n * 16, qrow * 8192 + h * 256);
  const u32x4 xa = ld16<0>(rX, l * 16, 0);
  const u32 xb = ld4<0>(rX, l * 4, 1024);
  const u32 xp = ld4<0>(rX, kg * 4, qrow * 2), xq = ld4<0>(rX, 16, qrow * 2);
  const u32 asw = (j >> 3) & 1;
  const u32 kvoffK = (l >> 3) * 256 + (l & 7) * 16, kvoffV = kg * 512 + n * 16;
  K[0] = ld16<NT>(rK, kvoffK, kvbase + asw * 2048);
  const u32 tag = *(const volatile __attribute__((address_space(4))) u32*)(u64)ws * 1664525u + 1013904223u;
  SB();
  const u32 mbit = (n & 1) ? 0xffff0000u : 0x0000ffffu, mr = (n & 7) >> 1;
  const u32x4 mask = {mr == 0 ? mbit : 0u, mr == 1 ? mbit : 0u, mr == 2 ? mbit : 0u, mr == 3 ? mbit : 0u};
  u32 lowhalf = kg < 2 ? ~0u : 0u;
  asm volatile("" : "+v"(lowhalf));
  STAMP(1);
  ARRIVED(Q[0]); K[1] = ld16<NT>(rK, kvoffK + 128, kvbase + asw * 2048); SB();
  ARRIVED(Q[1]); K[2] = ld16<NT>(rK, kvoffK, kvbase + (asw ^ 1) * 2048); SB();
  ARRIVED(Q[2]); K[3] = ld16<NT>(rK, kvoffK + 128, kvbase + (asw ^ 1) * 2048); SB();
  STAMP(2);
  {
    float s1 = blo(xb) + bhi(xb), s2 = blo(xb) * blo(xb) + bhi(xb) * bhi(xb);
#pragma unroll
    for (int i = 0; i < 4; ++i) { const float a = blo(xa[i]), c = bhi(xa[i]); s1 += a + c; s2 += a * a + c * c; }
    float mean_b, rs;
    ln_stats(wave_add(s1), wave_add(s2), mean_b, rs);
    const float x2 = (kg & 1) ? bhi(xq) : blo(xq);
    const float n0 = rbf(blo(xp) - mean_b) * rs, n1 = rbf(bhi(xp) - mean_b) * rs, n2 = rbf(x2 - mean_b) * rs;
    f32x4 acc = {0, 0, 0, 0};
    acc = mfma(diag(pk(n0, n0), mask), Q[0], acc);
    acc = mfma(diag(pk(n1, n1), mask), Q[1], acc);
    acc = mfma(diag(pk(n2, n2) & lowhalf, mask), Q[2], acc);
    if (l < 32) *(LAS f32x4*)(lds + L_RED + w * 512 + n * 32 + kg * 16) = acc;
  }
  wg_barrier();
  if (w == 0) {
    f32x2 a = *(LAS f32x2*)(lds + L_RED + l * 8);
#pragma unroll
    for (int i = 1; i < 8; ++i) a += *(LAS f32x2*)(lds + L_RED + i * 512 + l * 8);
    __builtin_amdgcn_raw_buffer_store_b128(u32x4{u_(a[0]), tag, u_(a[1]), tag}, rWS, l * 16, WS_QP + (h * 8 + s) * 1024, SC1);
    STAMP(3);
    float q0, q1;
    for (u32 spin = 0;; ++spin) {
      asm volatile("" ::: "memory");
      u32x4 g[8];
#pragma unroll
      for (int i = 0; i < 8; ++i) g[i] = ld16<SC1>(rWS, l * 16 + i * 1024, WS_QP + h * 8192);
      u32 bad = 0; q0 = 0; q1 = 0;
#pragma unroll
      for (int i = 0; i < 8; ++i) { bad |= (g[i][1] ^ tag) | (g[i][3] ^ tag); q0 += f_(g[i][0]); q1 += f_(g[i][2]); }
      if (__builtin_amdgcn_ballot_w64(bad != 0) == 0 || spin > SPIN_CAP) break;
    }
    *(LAS u32*)(lds + L_QS + l * 4) = pk(q0, q1);
    STAMP(4);
  }
  ARRIVED(K[0]); V[0] = ld16<NT>(rV, kvoffV, kvbase + asw * 2048); SB();
  ARRIVED(K[1]); V[1] = ld16<NT>(rV, kvoffV + 256, kvbase + asw * 2048); SB();
  ARRIVED(K[2]); V[2] = ld16<NT>(rV, kvoffV, kvbase + (asw ^ 1) * 2048); SB();
  ARRIVED(K[3]); V[3] = ld16<NT>(rV, kvoffV + 256, kvbase + (asw ^ 1) * 2048); SB();
  STAMP(5);
  const bool isWO = j < 10;
  const u32 fp = xcd + 8 * ((j - 10) >> 1), f = 2 * fp + ((j - 10) & 1);
  const bool isFF = j >= 10 && j < 18 && fp < 27;
  const bool isRD = j >= 18 && j < 23;
  const rsrc_t rA = mk(isWO ? wo : w1, isWO ? 4096u * D * 2 : isFF ? D * DFF * 2u : 0u);
  const rsrc_t rB = mk(isWO ? wo : w2, isWO ? 4096u * D * 2 : isFF ? D * DFF * 2u : 0u);
  const u32 voA = isWO ? (32 * (kg & 1) + 16 * (n >> 3) + (kg >> 1)) * 1280 + (n & 7) * 16 : (l >> 2) * 3456 + (l & 3) * 16;
  const u32 voB = isWO ? voA : l * 16;
  const u32 soA = isWO ? (xcd * 512 + w * 64) * 1280 + j * 128 : w * 80 * 3456 + f * 64, stA = isWO ? 2560u : 16 * 3456u;
  const u32 soB = isWO ? soA + 5 * 2560 : f * 40960 + w * 5120, stB = isWO ? 2560u : 1024u;
#define EXLD(i) ex[i] = (i) < 5 ? ld16<NT>(rA, voA, soA + (i) * stA) : ld16<NT>(rB, voB, soB + ((i) - 5) * stB)
  wg_barrier();
  STAMP(6);
  {
    const u32x4 qa = *(LAS u32x4*)(lds + L_QS + (l & 7) * 16), qb = *(LAS u32x4*)(lds + L_QS + 128 + (l & 7) * 16);
    float s0 = 0, s1 = 0;
#pragma unroll
    for (int i = 0; i < 4; ++i) { s0 = dot2(K[0][i], qa[i], s0); s1 = dot2(K[2][i], qa[i], s1); }
#pragma unroll
    for (int i = 0; i < 4; ++i) { s0 = dot2(K[1][i], qb[i], s0); s1 = dot2(K[3][i], qb[i], s1); }
    s0 = oct_add(s0); s1 = oct_add(s1);
    const u32 sr = pk(s0, s1);
    const u32 sc = pk(blo(sr) * 0.08838834764831845f, bhi(sr) * 0.08838834764831845f);
    s0 = blo(sc); s1 = bhi(sc);
    float m = fmaxf(s0, s1);
    m = fmaxf(m, dpp<ROR8>(m)); m = sw32_max(sw16_max(m));
    const float p0 = __builtin_amdgcn_exp2f((s0 - m) * 1.4426950408889634f), p1 = __builtin_amdgcn_exp2f((s1 - m) * 1.4426950408889634f);
    const u32 d0 = pk(p0, p0), d1 = pk(p1, p1);
    const u32x4 A0 = diag(dppu<BC + 0>(d0), mask), A1 = diag(dppu<BC + 8>(d0), mask), A2 = diag(dppu<BC + 0>(d1), mask), A3 = diag(dppu<BC + 8>(d1), mask);
    const float lsum = xoct_add(p0 + p1);
    if (l == 0) *(LAS f32x2*)(lds + L_ML + w * 8) = f32x2{m, lsum};
    STAMP(7);
    f32x4 acc = {0, 0, 0, 0};
    ARRIVED(V[0]);
#pragma unroll
    for (int i = 0; i < EX0; ++i) EXLD(i);
    SB(); acc = mfma(A0, V[0], acc);
    ARRIVED(V[1]);
#pragma unroll
    for (int i = EX0; i < EX0 + EX1; ++i) EXLD(i);
    SB(); acc = mfma(A1, V[1], acc);
    ARRIVED(V[2]);
#pragma unroll
    for (int i = EX0 + EX1; i < EX0 + EX1 + EX2; ++i) EXLD(i);
    SB(); acc = mfma(A2, V[2], acc);
    ARRIVED(V[3]);
    acc = mfma(A3, V[3], acc);
#pragma unroll
    for (int i = EX0 + EX1 + EX2; i < 8; ++i) EXLD(i);
    SB();
    if (l < 32) *(LAS f32x4*)(lds + L_RED + w * 512 + n * 32 + kg * 16) = acc;
  }
  STAMP(8);
  wg_barrier();
  if (w == 0) {
    const f32x2 ml = *(LAS f32x2*)(lds + L_ML + (l & 7) * 8);
    f32x2 o[8];
#pragma unroll
    for (int i = 0; i < 8; ++i) o[i] = *(LAS f32x2*)(lds + L_RED + i * 512 + l * 8);
    const float M = oct_max(ml[0]);
    const float e = __builtin_amdgcn_exp2f((ml[0] - M) * 1.4426950408889634f);
    const float L = oct_add(e * ml[1]);
    float o0 = dpp<BC + 0>(e) * o[0][0], o1 = dpp<BC + 0>(e) * o[0][1];
#define MRG(i) { const float e_ = dpp<BC + i>(e); o0 = fmaf(e_, o[i][0], o0); o1 = fmaf(e_, o[i][1], o1); }
    MRG(1) MRG(2) MRG(3) MRG(4) MRG(5) MRG(6) MRG(7)
#undef MRG
    __builtin_amdgcn_raw_buffer_store_b128(u32x4{u_(o0), tag, u_(o1), tag}, rWS, l * 16, WS_OP + (h * 8 + s) * 1024, SC1);
    if (l == 0) __builtin_amdgcn_raw_buffer_store_b128(u32x4{u_(M), tag, u_(L), tag}, rWS, 0, WS_ML + (h * 8 + s) * 16, SC1);
  }
  STAMP(9);
  if (isWO) {
    const u32 hh = xcd * 4 + (w >> 1), sg = l >> 5;
    float val;
    for (u32 spin = 0;; ++spin) {
      asm volatile("" ::: "memory");
      u32x4 g[4];
      const u32x4 ml = ld16<SC1>(rWS, ((l & 7) ^ sg) * 16, WS_ML + hh * 128);
#pragma unroll
      for (int i = 0; i < 4; ++i) g[i] = ld16<SC1>(rWS, sg * 1024 + (l & 31) * 16 + i * 2048, WS_OP + hh * 8192 + (w & 1) * 512);
      u32 bad = (ml[1] ^ tag) | (ml[3] ^ tag);
#pragma unroll
      for (int i = 0; i < 4; ++i) bad |= (g[i][1] ^ tag) | (g[i][3] ^ tag);
      const float M = oct_max(f_(ml[0]));
      const float e = __builtin_amdgcn_exp2f((f_(ml[0]) - M) * 1.4426950408889634f);
      const float L = oct_add(e * f_(ml[2]));
      const float wt = e * __builtin_amdgcn_rcpf(L);
      float o0 = dpp<BC + 0>(wt) * f_(g[0][0]), o1 = dpp<BC + 0>(wt) * f_(g[0][2]);
#define MRG(i) { const float e_ = dpp<BC + 2 * i>(wt); o0 = fmaf(e_, f_(g[i][0]), o0); o1 = fmaf(e_, f_(g[i][2]), o1); }
      MRG(1) MRG(2) MRG(3)
#undef MRG
      auto sw = __builtin_amdgcn_permlane32_swap(u_(o0), u_(o1), false, false);
      val = f_(sw[0]) + f_(sw[1]);
      if (__builtin_amdgcn_ballot_w64(bad != 0) == 0 || spin > SPIN_CAP) break;
    }
    STAMP(10);
    const u32 dup = pk(val, val);
    f32x4 acc = {0, 0, 0, 0};
#define WOS(t) { u32 a_ = __builtin_amdgcn_update_dpp(0u, dup, BC + t, 0xf, 0x3, false); a_ = __builtin_amdgcn_update_dpp(a_, dup, BC + 8 + t, 0xf, 0xc, false); \
                 acc = mfma(diag(a_, mask), ex[t], acc); }
    WOS(0) WOS(1) WOS(2) WOS(3) WOS(4) WOS(5) WOS(6) WOS(7)
#undef WOS
    if ((kg >> 1) == (n >> 3)) *(LAS f32x4*)(lds + L_RED + w * 512 + (kg >> 1) * 256 + (n & 7) * 32 + (kg & 1) * 16) = acc;
    STAMP(11);
    wg_barrier();
    if (w == 0) {
      float y = *(LAS float*)(lds + L_RED + l * 4);
#pragma unroll
      for (int i = 1; i < 16; ++i) y += *(LAS float*)(lds + L_RED + i * 256 + l * 4);
      __builtin_amdgcn_raw_buffer_store_b64(u32x2{u_(y), tag}, rWS, l * 8, WS_Y + xcd * 5120 + j * 512, SC1);
    }
    STAMP(12);
  } else if (isFF) {
    EXLD(8); EXLD(9);
    const u32 xh = ld4<0>(rX, l * 4, w * 256);
#pragma unroll
    for (int i = 0; i < 5; ++i) *(LAS u32x4*)(lds + L_W1 + w * 5120 + i * 1024 + l * 16) = ex[i];
#pragma unroll
    for (int i = 0; i < 5; ++i) *(LAS u32x4*)(lds + L_W2 + w * 5120 + i * 1024 + l * 16) = ex[5 + i];
    wg_barrier();
    u32x4 F1[10], F2[5];
    f32x4 cs = {0, 0, 0, 0};
    if (w < 4) {
      LAS char* p = lds + L_W1 + (w >> 1) * 20480 + (8 * kg + (n >> 2)) * 64 + (w & 1) * 32 + (n & 3) * 8;
#pragma unroll
      for (int i = 0; i < 10; ++i) { const u32x2 a = trrd(p + i * 2048), c = trrd(p + i * 2048 + 256); F1[i] = u32x4{a[0], a[1], c[0], c[1]}; }
#pragma unroll
      for (int i = 0; i < 10; ++i) cs = mfma(u32x4{0x3f803f80u, 0x3f803f80u, 0x3f803f80u, 0x3f803f80u}, F1[i], cs);
    }
    {
      LAS char* p = lds + L_W2 + (8 * kg + (n >> 2)) * 1280 + w * 160 + (n & 3) * 8;
#pragma unroll
      for (int i = 0; i < 5; ++i) { const u32x2 a = trrd(p + i * 32), c = trrd(p + i * 32 + 5120); F2[i] = u32x4{a[0], a[1], c[0], c[1]}; }
    }
    STAMP(10);
    if (w < 5) {
      float y0, y1;
      for (u32 spin = 0;; ++spin) {
        asm volatile("" ::: "memory");
        u32x4 g[8];
#pragma unroll
        for (int i = 0; i < 8; ++i) g[i] = ld16<SC1>(rWS, l * 16, WS_Y + w * 1024 + i * 5120);
        u32 bad = 0; y0 = 0; y1 = 0;
#pragma unroll
        for (int i = 0; i < 8; ++i) { bad |= (g[i][1] ^ tag) | (g[i][3] ^ tag); y0 += f_(g[i][0]); y1 += f_(g[i][2]); }
        if (__builtin_amdgcn_ballot_w64(bad != 0) == 0 || spin > SPIN_CAP) break;
      }
      STAMP(11);
      const u32 at = pk(y0, y1);
      const u32 hp = pk(blo(xh) + blo(at), bhi(xh) + bhi(at));
      *(LAS u32*)(lds + L_HS + w * 256 + l * 4) = hp;
      const float h0 = blo(hp), h1 = bhi(hp);
      const float s1 = wave_add(h0 + h1), s2 = wave_add(h0 * h0 + h1 * h1);
      if (l == 0) *(LAS f32x2*)(lds + L_STAT + w * 8) = f32x2{s1, s2};
    }
    wg_barrier();
    if (w < 4) {
      u32x4 A[10];
#pragma unroll
      for (int i = 0; i < 10; ++i) A[i] = *(LAS u32x4*)(lds + L_HS + (w >> 1) * 640 + i * 64 + kg * 16);
      const f32x4 sa = *(LAS f32x4*)(lds + L_STAT), sb = *(LAS f32x4*)(lds + L_STAT + 16);
      const f32x2 sc = *(LAS f32x2*)(lds + L_STAT + 32);
      f32x4 acc = {0, 0, 0, 0};
#pragma unroll
      for (int i = 0; i < 10; ++i) acc = mfma(A[i], F1[i], acc);
      float mean_b, rs;
      ln_stats((sa[0] + sa[2]) + (sb[0] + sb[2]) + sc[0], (sa[1] + sa[3]) + (sb[1] + sb[3]) + sc[1], mean_b, rs);
      const float z = (acc[0] - mean_b * cs[0]) * rs;
      if (l < 16) *(LAS float*)(lds + L_ZP + (w >> 1) * 128 + (w & 1) * 64 + l * 4) = z;
    }
    STAMP(12);
    wg_barrier();
    {
      const f32x4 z0 = *(LAS f32x4*)(lds + L_ZP + kg * 32) + *(LAS f32x4*)(lds + L_ZP + 128 + kg * 32);
      const f32x4 z1 = *(LAS f32x4*)(lds + L_ZP + kg * 32 + 16) + *(LAS f32x4*)(lds + L_ZP + 128 + kg * 32 + 16);
      const u32x4 A = {pk(fmaxf(z0[0], 0.f), fmaxf(z0[1], 0.f)), pk(fmaxf(z0[2], 0.f), fmaxf(z0[3], 0.f)),
                       pk(fmaxf(z1[0], 0.f), fmaxf(z1[1], 0.f)), pk(fmaxf(z1[2], 0.f), fmaxf(z1[3], 0.f))};
      f32x4 r[5];
#pragma unroll
      for (int i = 0; i < 5; ++i) r[i] = mfma(A, F2[i], f32x4{0, 0, 0, 0});
      const float v = kg == 0 ? r[0][0] : kg == 1 ? r[1][0] : kg == 2 ? r[2][0] : r[3][0];
      __builtin_amdgcn_raw_buffer_store_b64(u32x2{u_(v), tag}, rWS, l * 8, WS_P4 + f * 5120 + w * 640, SC1);
      if (l < 16) __builtin_amdgcn_raw_buffer_store_b64(u32x2{u_(r[4][0]), tag}, rWS, l * 8 + 512, WS_P4 + f * 5120 + w * 640, SC1);
    }
    STAMP(13);
  } else if (isRD && w == 0) {
    const u32 r = xcd * 5 + (j - 18);
    const rsrc_t rO = mk(out, D * 2);
    const u32 xh = ld4<0>(rX, (l & 7) * 4, r * 32);
    float y0, y1;
    for (u32 spin = 0;; ++spin) {
      asm volatile("" ::: "memory");
      const u32x4 g = ld16<SC1>(rWS, (l >> 3) * 5120 + (l & 7) * 16, WS_Y + r * 128);
      y0 = f_(g[0]); y1 = f_(g[2]);
      if (__builtin_amdgcn_ballot_w64(((g[1] ^ tag) | (g[3] ^ tag)) != 0) == 0 || spin > SPIN_CAP) break;
    }
    STAMP(10);
    const u32 at = pk(xoct_add(y0), xoct_add(y1));
    const u32 hp = pk(blo(xh) + blo(at), bhi(xh) + bhi(at));
    const bool tail = l >= 48;
    for (u32 spin = 0;; ++spin) {
      asm volatile("" ::: "memory");
      u32x4 g[7];
#pragma unroll
      for (int i = 0; i < 6; ++i) g[i] = ld16<SC1>(rWS, (l >> 3) * 5120 + (l & 7) * 16, WS_P4 + r * 128 + i * 40960);
      g[6] = ld16<SC1>(rWS, (tail ? 5 : (l >> 3)) * 5120 + (l & 7) * 16, WS_P4 + r * 128 + 6 * 40960);
      u32 bad = 0; y0 = tail ? 0.f : f_(g[6][0]); y1 = tail ? 0.f : f_(g[6][2]);
#pragma unroll
      for (int i = 0; i < 7; ++i) bad |= (g[i][1] ^ tag) | (g[i][3] ^ tag);
#pragma unroll
      for (int i = 0; i < 6; ++i) { y0 += f_(g[i][0]); y1 += f_(g[i][2]); }
      if (__builtin_amdgcn_ballot_w64(bad != 0) == 0 || spin > SPIN_CAP) break;
    }
    STAMP(11);
    const u32 ff = pk(xoct_add(y0), xoct_add(y1));
    const u32 o = pk(blo(hp) + blo(ff), bhi(hp) + bhi(ff));
    if (l < 8) __builtin_amdgcn_raw_buffer_store_b32(o, rO, l * 4, r * 32, SC1);
    if (r == 0 && l == 0) __builtin_amdgcn_raw_buffer_store_b32(tag, rWS, 0, 0, SC1);
    STAMP(12);
  }
  FLUSH_STAMPS();
}

extern "C" void kernel_launch(void* const* d_in, const int* in_sizes, int n_in, void* d_out, int out_size, void* d_ws, size_t ws_size,
                              hipStream_t stream) {
  fused_block<<<256, 512, 0, stream>>>(d_in[0], d_in[1], d_in[2], d_in[3], d_in[4], d_in[6], d_in[7], d_out, d_ws);
}
#define ROLE_OF(b) (((b) >> 3) < 10 ? 1 : (((b) >> 3) < 18 && (((b) & 7) + 8 * ((((b) >> 3) - 10) >> 1)) < 27) ? 2 : (((b) >> 3) >= 18 && ((b) >> 3) < 23) ? 3 : 0)
```

```cpp
#define STAMPS 1
#define PROBE 0
#define PROBE_IDX 0
#define PROBE_ROLE -1
#include <hip/hip_runtime.h>
#include <hip/hip_bf16.h>
#include <stdint.h>

typedef unsigned u32;
typedef unsigned long long u64;
typedef u32 u32x2 __attribute__((ext_vector_type(2)));
typedef u32 u32x4 __attribute__((ext_vector_type(4)));
typedef float f32x2 __attribute__((ext_vector_type(2)));
typedef float f32x4 __attribute__((ext_vector_type(4)));
typedef __bf16 bf16x2 __attribute__((ext_vector_type(2)));
typedef __bf16 bf16x8 __attribute__((ext_vector_type(8)));
typedef short s16x4 __attribute__((ext_vector_type(4)));
typedef __amdgpu_buffer_rsrc_t rsrc_t;
#define LAS __attribute__((address_space(3)))
#define DEV __device__ __forceinline__

constexpr int D = 640, SK = 4096, WIN = 1024, DFF = 1728;
constexpr u32 WS_QP = 4096;
constexpr u32 WS_OP = WS_QP + 32 * 8 * 1024;
constexpr u32 WS_ML = WS_OP + 32 * 8 * 1024;
constexpr u32 WS_Y = WS_ML + 32 * 8 * 16;
constexpr u32 WS_P4 = WS_Y + 8 * 640 * 8;
constexpr u32 WS_END = WS_P4 + 54 * 640 * 8;
constexpr u32 WS_ST = 16u << 20;
constexpr int NT = 2, SC1 = 16;
constexpr u32 SPIN_CAP = 1u << 18;
constexpr u32 L_RED = 0, L_QS = 4096, L_ML = L_QS + 256, L_HS = L_ML + 64, L_STAT = L_HS + 1280, L_ZP = L_STAT + 64,
              L_W1 = 8192, L_W2 = L_W1 + 40960, L_END = L_W2 + 40960;

#define EX0 2
#define EX1 2
#define EX2 2

DEV rsrc_t mk(const void* p, u32 bytes) { return __builtin_amdgcn_make_buffer_rsrc((void*)p, (short)0, (int)bytes, 0x00020000); }
template <int AUX> DEV u32x4 ld16(rsrc_t r, u32 v, u32 s) { return __builtin_amdgcn_raw_buffer_load_b128(r, v, s, AUX); }
template <int AUX> DEV u32 ld4(rsrc_t r, u32 v, u32 s) { return __builtin_amdgcn_raw_buffer_load_b32(r, v, s, AUX); }
DEV float f_(u32 u) { return __builtin_bit_cast(float, u); }
DEV u32 u_(float f) { return __builtin_bit_cast(u32, f); }
DEV float blo(u32 u) { return f_(u << 16); }
DEV float bhi(u32 u) { return f_(u & 0xffff0000u); }
DEV u32 pk(float lo, float hi) { f32x2 v = {lo, hi}; return __builtin_bit_cast(u32, __builtin_convertvector(v, bf16x2)); }
DEV float rbf(float f) { return blo(pk(f, f)); }
template <int C> DEV float dpp(float x) { return f_(__builtin_amdgcn_mov_dpp(u_(x), C, 0xf, 0xf, true)); }
template <int C> DEV u32 dppu(u32 x) { return __builtin_amdgcn_mov_dpp(x, C, 0xf, 0xf, true); }
constexpr int XOR1 = 0xB1, XOR2 = 0x4E, HMIR = 0x141, ROR8 = 0x128, BC = 0x150;
DEV float sw16_add(float x) { auto s = __builtin_amdgcn_permlane16_swap(u_(x), u_(x), false, false); return f_(s[0]) + f_(s[1]); }
DEV float sw32_add(float x) { auto s = __builtin_amdgcn_permlane32_swap(u_(x), u_(x), false, false); return f_(s[0]) + f_(s[1]); }
DEV float sw16_max(float x) { auto s = __builtin_amdgcn_permlane16_swap(u_(x), u_(x), false, false); return fmaxf(f_(s[0]), f_(s[1])); }
DEV float sw32_max(float x) { auto s = __builtin_amdgcn_permlane32_swap(u_(x), u_(x), false, false); return fmaxf(f_(s[0]), f_(s[1])); }
DEV float oct_add(float x) { x += dpp<XOR1>(x); x += dpp<XOR2>(x); return x + dpp<HMIR>(x); }
DEV float oct_max(float x) { x = fmaxf(x, dpp<XOR1>(x)); x = fmaxf(x, dpp<XOR2>(x)); return fmaxf(x, dpp<HMIR>(x)); }
DEV float xoct_add(float x) { x += dpp<ROR8>(x); return sw32_add(sw16_add(x)); }
DEV float wave_add(float x) { return xoct_add(oct_add(x)); }
DEV float dot2(u32 a, u32 b, float c) { return __builtin_amdgcn_fdot2_f32_bf16(__builtin_bit_cast(bf16x2, a), __builtin_bit_cast(bf16x2, b), c, false); }
DEV f32x4 mfma(u32x4 a, u32x4 b, f32x4 c) { return __builtin_amdgcn_mfma_f32_16x16x32_bf16(__builtin_bit_cast(bf16x8, a), __builtin_bit_cast(bf16x8, b), c, 0, 0, 0); }
DEV u32x4 diag(u32 dup, u32x4 m) { return u32x4{dup & m[0], dup & m[1], dup & m[2], dup & m[3]}; }
#define SB() __builtin_amdgcn_sched_barrier(0)
#define ARRIVED(v) do { asm volatile("" :: "v"(v)); SB(); } while (0)
DEV void wg_barrier() { asm volatile("s_waitcnt lgkmcnt(0)\n\ts_barrier" ::: "memory"); }
DEV u32x2 trrd(LAS char* p) { return __builtin_bit_cast(u32x2, __builtin_amdgcn_ds_read_tr16_b64_v4i16((LAS s16x4*)p)); }
DEV void ln_stats(float s1, float s2, float& mean_b, float& rs) {
  const float mean = s1 * (1.0f / D);
  const float var = fmaxf(s2 * (1.0f / D) - mean * mean, 0.0f);
  mean_b = rbf(mean);
  rs = __builtin_amdgcn_rcpf(rbf(__builtin_amdgcn_sqrtf(rbf(rbf(var) + 1e-5f))));
}
#define ROLE_OF(b) (((b) >> 3) < 10 ? 1 : (((b) >> 3) < 18 && (((b) & 7) + 8 * ((((b) >> 3) - 10) >> 1)) < 27) ? 2 : (((b) >> 3) >= 18 && ((b) >> 3) < 23) ? 3 : 0)
#define STAMP(i) do { u64 t_; SB(); asm volatile("s_memrealtime %0\n\ts_waitcnt lgkmcnt(0)" : "=s"(t_) :: "memory"); SB(); st[i] = (u32)t_; } while (0)
#define FLUSH_STAMPS() do { u32* sp_ = (u32*)((char*)ws + WS_ST); \
  if (l == 0) { for (int i_ = 0; i_ < 16; ++i_) __hip_atomic_store(sp_ + (b * 8 + w) * 16 + i_, st[i_], __ATOMIC_RELAXED, __HIP_MEMORY_SCOPE_AGENT); } \
  if (b == 255 && w == 0) { u64 t_; do { t_ = __builtin_amdgcn_s_memrealtime(); } while ((u32)t_ - st[0] < 4500u); \
    int mn_ = 0x7fffffff, v_ = PROBE == 2 ? 0x7fffffff : -0x7fffffff; \
    for (int k_ = l; k_ < 2048; k_ += 64) { const int e_ = (int)(__hip_atomic_load(sp_ + k_ * 16, __ATOMIC_RELAXED, __HIP_MEMORY_SCOPE_AGENT) - st[0]); mn_ = min(mn_, e_); \
      const u32 r_ = __hip_atomic_load(sp_ + k_ * 16 + PROBE_IDX, __ATOMIC_RELAXED, __HIP_MEMORY_SCOPE_AGENT); const int bb_ = k_ >> 3; \
      if (r_ != 0 && (PROBE_ROLE < 0 || ROLE_OF(bb_) == PROBE_ROLE)) { const int q_ = (int)(r_ - st[0]); v_ = PROBE == 2 ? min(v_, q_) : max(v_, q_); } } \
    for (int o_ = 32; o_ > 0; o_ >>= 1) { mn_ = min(mn_, __shfl_xor(mn_, o_)); const int y_ = __shfl_xor(v_, o_); v_ = PROBE == 2 ? min(v_, y_) : max(v_, y_); } \
    int d_ = PROBE == 0 ? 0 : v_ - mn_; d_ = max(0, min(d_, 4000)); \
    do { t_ = __builtin_amdgcn_s_memrealtime(); } while ((int)((u32)t_ - st[0]) - mn_ < 6000 + d_); } } while (0)

__global__ void __launch_bounds__(512, 2)
fused_block(const void* __restrict__ x, const void* __restrict__ kc, const void* __restrict__ vc, const void* __restrict__ wq,
            const void* __restrict__ wo, const void* __restrict__ w1, const void* __restrict__ w2, void* __restrict__ out, void* ws) {
  __shared__ __attribute__((aligned(16))) char lds_[L_END];
  LAS char* lds = (LAS char*)lds_;
  u32 st[16] = {0};
  const u32 b = blockIdx.x, xcd = b & 7, j = b >> 3, h = xcd * 4 + (j & 3), s = j >> 2;
  const u32 w = __builtin_amdgcn_readfirstlane(threadIdx.x >> 6), l = threadIdx.x & 63, n = l & 15, kg = l >> 4;
  STAMP(0);
  asm volatile("" :: "s"(x), "s"(kc), "s"(vc), "s"(wq), "s"(wo), "s"(w1), "s"(w2), "s"(out), "s"(ws));
  const rsrc_t rX = mk(x, D * 2), rQ = mk(wq, D * 4096 * 2), rWS = mk(ws, WS_END);
  const u32 kvbase = (h * SK + (SK - WIN) + s * 128 + ((w + j) & 7) * 16) * 256;
  const rsrc_t rK = mk(kc, 32u * SK * 256), rV = mk(vc, 32u * SK * 256);
  const u32 qrow = s * 80 + w * 10;
  u32x4 Q[3], K[4], V[4], ex[10];
  Q[0] = ld16<NT>(rQ, (2 * kg) * 8192 + n * 16, qrow * 8192 + h * 256);
  Q[1] = ld16<NT>(rQ, (2 * kg + 1) * 8192 + n * 16, qrow * 8192 + h * 256);
  Q[2] = ld16<NT>(rQ, (8 + (kg & 1)) * 8192 + n * 16, qrow * 8192 + h * 256);
  const u32x4 xa = ld16<0>(rX, l * 16, 0);
  const u32 xb = ld4<0>(rX, l * 4, 1024);
  const u32 xp = ld4<0>(rX, kg * 4, qrow * 2), xq = ld4<0>(rX, 16, qrow * 2);
  const u32 asw = (j >> 3) & 1;
  const u32 kvoffK = (l >> 3) * 256 + (l & 7) * 16, kvoffV = kg * 512 + n * 16;
  K[0] = ld16<NT>(rK, kvoffK, kvbase + asw * 2048);
  const u32 tag = *(const volatile __attribute__((address_space(4))) u32*)(u64)ws * 1664525u + 1013904223u;
  SB();
  const u32 mbit = (n & 1) ? 0xffff0000u : 0x0000ffffu, mr = (n & 7) >> 1;
  const u32x4 mask = {mr == 0 ? mbit : 0u, mr == 1 ? mbit : 0u, mr == 2 ? mbit : 0u, mr == 3 ? mbit : 0u};
  u32 lowhalf = kg < 2 ? ~0u : 0u;
  asm volatile("" : "+v"(lowhalf));
  STAMP(1);
  ARRIVED(Q[0]); K[1] = ld16<NT>(rK, kvoffK + 128, kvbase + asw * 2048); SB();
  ARRIVED(Q[1]); K[2] = ld16<NT>(rK, kvoffK, kvbase + (asw ^ 1) * 2048); SB();
  ARRIVED(Q[2]); K[3] = ld16<NT>(rK, kvoffK + 128, kvbase + (asw ^ 1) * 2048); SB();
  STAMP(2);
  {
    float s1 = blo(xb) + bhi(xb), s2 = blo(xb) * blo(xb) + bhi(xb) * bhi(xb);
#pragma unroll
    for (int i = 0; i < 4; ++i) { const float a = blo(xa[i]), c = bhi(xa[i]); s1 += a + c; s2 += a * a + c * c; }
    float mean_b, rs;
    ln_stats(wave_add(s1), wave_add(s2), mean_b, rs);
    const float x2 = (kg & 1) ? bhi(xq) : blo(xq);
    const float n0 = rbf(blo(xp) - mean_b) * rs, n1 = rbf(bhi(xp) - mean_b) * rs, n2 = rbf(x2 - mean_b) * rs;
    f32x4 acc = {0, 0, 0, 0};
    acc = mfma(diag(pk(n0, n0), mask), Q[0], acc);
    acc = mfma(diag(pk(n1, n1), mask), Q[1], acc);
    acc = mfma(diag(pk(n2, n2) & lowhalf, mask), Q[2], acc);
    if (l < 32) *(LAS f32x4*)(lds + L_RED + w * 512 + n * 32 + kg * 16) = acc;
  }
  wg_barrier();
  if (w == 0) {
    f32x2 a = *(LAS f32x2*)(lds + L_RED + l * 8);
#pragma unroll
    for (int i = 1; i < 8; ++i) a += *(LAS f32x2*)(lds + L_RED + i * 512 + l * 8);
    __builtin_amdgcn_raw_buffer_store_b128(u32x4{u_(a[0]), tag, u_(a[1]), tag}, rWS, l * 16, WS_QP + (h * 8 + s) * 1024, SC1);
    STAMP(3);
    float q0, q1;
    for (u32 spin = 0;; ++spin) {
      asm volatile("" ::: "memory");
      u32x4 g[8];
#pragma unroll
      for (int i = 0; i < 8; ++i) g[i] = ld16<SC1>(rWS, l * 16 + i * 1024, WS_QP + h * 8192);
      u32 bad = 0; q0 = 0; q1 = 0;
#pragma unroll
      for (int i = 0; i < 8; ++i) { bad |= (g[i][1] ^ tag) | (g[i][3] ^ tag); q0 += f_(g[i][0]); q1 += f_(g[i][2]); }
      if (__builtin_amdgcn_ballot_w64(bad != 0) == 0 || spin > SPIN_CAP) break;
    }
    *(LAS u32*)(lds + L_QS + l * 4) = pk(q0, q1);
    STAMP(4);
  }
  ARRIVED(K[0]); V[0] = ld16<NT>(rV, kvoffV, kvbase + asw * 2048); SB();
  ARRIVED(K[1]); V[1] = ld16<NT>(rV, kvoffV + 256, kvbase + asw * 2048); SB();
  ARRIVED(K[2]); V[2] = ld16<NT>(rV, kvoffV, kvbase + (asw ^ 1) * 2048); SB();
  ARRIVED(K[3]); V[3] = ld16<NT>(rV, kvoffV + 256, kvbase + (asw ^ 1) * 2048); SB();
  STAMP(5);
  const bool isWO = j < 10;
  const u32 fp = xcd + 8 * ((j - 10) >> 1), f = 2 * fp + ((j - 10) & 1);
  const bool isFF = j >= 10 && j < 18 && fp < 27;
  const bool isRD = j >= 18 && j < 23;
  const rsrc_t rA = mk(isWO ? wo : w1, isWO ? 4096u * D * 2 : isFF ? D * DFF * 2u : 0u);
  const rsrc_t rB = mk(isWO ? wo : w2, isWO ? 4096u * D * 2 : isFF ? D * DFF * 2u : 0u);
  const u32 voA = isWO ? (32 * (kg & 1) + 16 * (n >> 3) + (kg >> 1)) * 1280 + (n & 7) * 16 : (l >> 2) * 3456 + (l & 3) * 16;
  const u32 voB = isWO ? voA : l * 16;
  const u32 soA = isWO ? (xcd * 512 + w * 64) * 1280 + j * 128 : w * 80 * 3456 + f * 64, stA = isWO ? 2560u : 16 * 3456u;
  const u32 soB = isWO ? soA + 5 * 2560 : f * 40960 + w * 5120, stB = isWO ? 2560u : 1024u;
#define EXLD(i) ex[i] = (i) < 5 ? ld16<NT>(rA, voA, soA + (i) * stA) : ld16<NT>(rB, voB, soB + ((i) - 5) * stB)
  wg_barrier();
  STAMP(6);
  {
    const u32x4 qa = *(LAS u32x4*)(lds + L_QS + (l & 7) * 16), qb = *(LAS u32x4*)(lds + L_QS + 128 + (l & 7) * 16);
    float s0 = 0, s1 = 0;
#pragma unroll
    for (int i = 0; i < 4; ++i) { s0 = dot2(K[0][i], qa[i], s0); s1 = dot2(K[2][i], qa[i], s1); }
#pragma unroll
    for (int i = 0; i < 4; ++i) { s0 = dot2(K[1][i], qb[i], s0); s1 = dot2(K[3][i], qb[i], s1); }
    s0 = oct_add(s0); s1 = oct_add(s1);
    const u32 sr = pk(s0, s1);
    const u32 sc = pk(blo(sr) * 0.08838834764831845f, bhi(sr) * 0.08838834764831845f);
    s0 = blo(sc); s1 = bhi(sc);
    float m = fmaxf(s0, s1);
    m = fmaxf(m, dpp<ROR8>(m)); m = sw32_max(sw16_max(m));
    const float p0 = __builtin_amdgcn_exp2f((s0 - m) * 1.4426950408889634f), p1 = __builtin_amdgcn_exp2f((s1 - m) * 1.4426950408889634f);
    const u32 d0 = pk(p0, p0), d1 = pk(p1, p1);
    const u32x4 A0 = diag(dppu<BC + 0>(d0), mask), A1 = diag(dppu<BC + 8>(d0), mask), A2 = diag(dppu<BC + 0>(d1), mask), A3 = diag(dppu<BC + 8>(d1), mask);
    const float lsum = xoct_add(p0 + p1);
    if (l == 0) *(LAS f32x2*)(lds + L_ML + w * 8) = f32x2{m, lsum};
    STAMP(7);
    f32x4 acc = {0, 0, 0, 0};
    ARRIVED(V[0]);
#pragma unroll
    for (int i = 0; i < EX0; ++i) EXLD(i);
    SB(); acc = mfma(A0, V[0], acc);
    ARRIVED(V[1]);
#pragma unroll
    for (int i = EX0; i < EX0 + EX1; ++i) EXLD(i);
    SB(); acc = mfma(A1, V[1], acc);
    ARRIVED(V[2]);
#pragma unroll
    for (int i = EX0 + EX1; i < EX0 + EX1 + EX2; ++i) EXLD(i);
    SB(); acc = mfma(A2, V[2], acc);
    ARRIVED(V[3]);
    acc = mfma(A3, V[3], acc);
#pragma unroll
    for (int i = EX0 + EX1 + EX2; i < 8; ++i) EXLD(i);
    SB();
    if (l < 32) *(LAS f32x4*)(lds + L_RED + w * 512 + n * 32 + kg * 16) = acc;
  }
  STAMP(8);
  wg_barrier();
  if (w == 0) {
    const f32x2 ml = *(LAS f32x2*)(lds + L_ML + (l & 7) * 8);
    f32x2 o[8];
#pragma unroll
    for (int i = 0; i < 8; ++i) o[i] = *(LAS f32x2*)(lds + L_RED + i * 512 + l * 8);
    const float M = oct_max(ml[0]);
    const float e = __builtin_amdgcn_exp2f((ml[0] - M) * 1.4426950408889634f);
    const float L = oct_add(e * ml[1]);
    float o0 = dpp<BC + 0>(e) * o[0][0], o1 = dpp<BC + 0>(e) * o[0][1];
#define MRG(i) { const float e_ = dpp<BC + i>(e); o0 = fmaf(e_, o[i][0], o0); o1 = fmaf(e_, o[i][1], o1); }
    MRG(1) MRG(2) MRG(3) MRG(4) MRG(5) MRG(6) MRG(7)
#undef MRG
    __builtin_amdgcn_raw_buffer_store_b128(u32x4{u_(o0), tag, u_(o1), tag}, rWS, l * 16, WS_OP + (h * 8 + s) * 1024, SC1);
    if (l == 0) __builtin_amdgcn_raw_buffer_store_b128(u32x4{u_(M), tag, u_(L), tag}, rWS, 0, WS_ML + (h * 8 + s) * 16, SC1);
  }
  STAMP(9);
  if (isWO) {
    const u32 hh = xcd * 4 + (w >> 1), sg = l >> 5;
    float val;
    for (u32 spin = 0;; ++spin) {
      asm volatile("" ::: "memory");
      u32x4 g[4];
      const u32x4 ml = ld16<SC1>(rWS, ((l & 7) ^ sg) * 16, WS_ML + hh * 128);
#pragma unroll
      for (int i = 0; i < 4; ++i) g[i] = ld16<SC1>(rWS, sg * 1024 + (l & 31) * 16 + i * 2048, WS_OP + hh * 8192 + (w & 1) * 512);
      u32 bad = (ml[1] ^ tag) | (ml[3] ^ tag);
#pragma unroll
      for (int i = 0; i < 4; ++i) bad |= (g[i][1] ^ tag) | (g[i][3] ^ tag);
      const float M = oct_max(f_(ml[0]));
      const float e = __builtin_amdgcn_exp2f((f_(ml[0]) - M) * 1.4426950408889634f);
      const float L = oct_add(e * f_(ml[2]));
      const float wt = e * __builtin_amdgcn_rcpf(L);
      float o0 = dpp<BC + 0>(wt) * f_(g[0][0]), o1 = dpp<BC + 0>(wt) * f_(g[0][2]);
#define MRG(i) { const float e_ = dpp<BC + 2 * i>(wt); o0 = fmaf(e_, f_(g[i][0]), o0); o1 = fmaf(e_, f_(g[i][2]), o1); }
      MRG(1) MRG(2) MRG(3)
#undef MRG
      auto sw = __builtin_amdgcn_permlane32_swap(u_(o0), u_(o1), false, false);
      val = f_(sw[0]) + f_(sw[1]);
      if (__builtin_amdgcn_ballot_w64(bad != 0) == 0 || spin > SPIN_CAP) break;
    }
    STAMP(10);
    const u32 dup = pk(val, val);
    f32x4 acc = {0, 0, 0, 0};
#define WOS(t) { u32 a_ = __builtin_amdgcn_update_dpp(0u, dup, BC + t, 0xf, 0x3, false); a_ = __builtin_amdgcn_update_dpp(a_, dup, BC + 8 + t, 0xf, 0xc, false); \
                 acc = mfma(diag(a_, mask), ex[t], acc); }
    WOS(0) WOS(1) WOS(2) WOS(3) WOS(4) WOS(5) WOS(6) WOS(7)
#undef WOS
    if ((kg >> 1) == (n >> 3)) *(LAS f32x4*)(lds + L_RED + w * 512 + (kg >> 1) * 256 + (n & 7) * 32 + (kg & 1) * 16) = acc;
    STAMP(11);
    wg_barrier();
    if (w == 0) {
      float y = *(LAS float*)(lds + L_RED + l * 4);
#pragma unroll
      for (int i = 1; i < 16; ++i) y += *(LAS float*)(lds + L_RED + i * 256 + l * 4);
      __builtin_amdgcn_raw_buffer_store_b64(u32x2{u_(y), tag}, rWS, l * 8, WS_Y + xcd * 5120 + j * 512, SC1);
    }
    STAMP(12);
  } else if (isFF) {
    EXLD(8); EXLD(9);
    const u32 xh = ld4<0>(rX, l * 4, w * 256);
#pragma unroll
    for (int i = 0; i < 5; ++i) *(LAS u32x4*)(lds + L_W1 + w * 5120 + i * 1024 + l * 16) = ex[i];
#pragma unroll
    for (int i = 0; i < 5; ++i) *(LAS u32x4*)(lds + L_W2 + w * 5120 + i * 1024 + l * 16) = ex[5 + i];
    wg_barrier();
    u32x4 F1[10], F2[5];
    f32x4 cs = {0, 0, 0, 0};
    if (w < 4) {
      LAS char* p = lds + L_W1 + (w >> 1) * 20480 + (8 * kg + (n >> 2)) * 64 + (w & 1) * 32 + (n & 3) * 8;
#pragma unroll
      for (int i = 0; i < 10; ++i) { const u32x2 a = trrd(p + i * 2048), c = trrd(p + i * 2048 + 256); F1[i] = u32x4{a[0], a[1], c[0], c[1]}; }
#pragma unroll
      for (int i = 0; i < 10; ++i) cs = mfma(u32x4{0x3f803f80u, 0x3f803f80u, 0x3f803f80u, 0x3f803f80u}, F1[i], cs);
    }
    {
      LAS char* p = lds + L_W2 + (8 * kg + (n >> 2)) * 1280 + w * 160 + (n & 3) * 8;
#pragma unroll
      for (int i = 0; i < 5; ++i) { const u32x2 a = trrd(p + i * 32), c = trrd(p + i * 32 + 5120); F2[i] = u32x4{a[0], a[1], c[0], c[1]}; }
    }
    STAMP(10);
    if (w < 5) {
      float y0, y1;
      for (u32 spin = 0;; ++spin) {
        asm volatile("" ::: "memory");
        u32x4 g[8];
#pragma unroll
        for (int i = 0; i < 8; ++i) g[i] = ld16<SC1>(rWS, l * 16, WS_Y + w * 1024 + i * 5120);
        u32 bad = 0; y0 = 0; y1 = 0;
#pragma unroll
        for (int i = 0; i < 8; ++i) { bad |= (g[i][1] ^ tag) | (g[i][3] ^ tag); y0 += f_(g[i][0]); y1 += f_(g[i][2]); }
        if (__builtin_amdgcn_ballot_w64(bad != 0) == 0 || spin > SPIN_CAP) break;
      }
      STAMP(11);
      const u32 at = pk(y0, y1);
      const u32 hp = pk(blo(xh) + blo(at), bhi(xh) + bhi(at));
      *(LAS u32*)(lds + L_HS + w * 256 + l * 4) = hp;
      const float h0 = blo(hp), h1 = bhi(hp);
      const float s1 = wave_add(h0 + h1), s2 = wave_add(h0 * h0 + h1 * h1);
      if (l == 0) *(LAS f32x2*)(lds + L_STAT + w * 8) = f32x2{s1, s2};
    }
    wg_barrier();
    if (w < 4) {
      u32x4 A[10];
#pragma unroll
      for (int i = 0; i < 10; ++i) A[i] = *(LAS u32x4*)(lds + L_HS + (w >> 1) * 640 + i * 64 + kg * 16);
      const f32x4 sa = *(LAS f32x4*)(lds + L_STAT), sb = *(LAS f32x4*)(lds + L_STAT + 16);
      const f32x2 sc = *(LAS f32x2*)(lds + L_STAT + 32);
      f32x4 acc = {0, 0, 0, 0};
#pragma unroll
      for (int i = 0; i < 10; ++i) acc = mfma(A[i], F1[i], acc);
      float mean_b, rs;
      ln_stats((sa[0] + sa[2]) + (sb[0] + sb[2]) + sc[0], (sa[1] + sa[3]) + (sb[1] + sb[3]) + sc[1], mean_b, rs);
      const float z = (acc[0] - mean_b * cs[0]) * rs;
      if (l < 16) *(LAS float*)(lds + L_ZP + (w >> 1) * 128 + (w & 1) * 64 + l * 4) = z;
    }
    STAMP(12);
    wg_barrier();
    {
      const f32x4 z0 = *(LAS f32x4*)(lds + L_ZP + kg * 32) + *(LAS f32x4*)(lds + L_ZP + 128 + kg * 32);
      const f32x4 z1 = *(LAS f32x4*)(lds + L_ZP + kg * 32 + 16) + *(LAS f32x4*)(lds + L_ZP + 128 + kg * 32 + 16);
      const u32x4 A = {pk(fmaxf(z0[0], 0.f), fmaxf(z0[1], 0.f)), pk(fmaxf(z0[2], 0.f), fmaxf(z0[3], 0.f)),
                       pk(fmaxf(z1[0], 0.f), fmaxf(z1[1], 0.f)), pk(fmaxf(z1[2], 0.f), fmaxf(z1[3], 0.f))};
      f32x4 r[5];
#pragma unroll
      for (int i = 0; i < 5; ++i) r[i] = mfma(A, F2[i], f32x4{0, 0, 0, 0});
      const float v = kg == 0 ? r[0][0] : kg == 1 ? r[1][0] : kg == 2 ? r[2][0] : r[3][0];
      __builtin_amdgcn_raw_buffer_store_b64(u32x2{u_(v), tag}, rWS, l * 8, WS_P4 + f * 5120 + w * 640, SC1);
      if (l < 16) __builtin_amdgcn_raw_buffer_store_b64(u32x2{u_(r[4][0]), tag}, rWS, l * 8 + 512, WS_P4 + f * 5120 + w * 640, SC1);
    }
    STAMP(13);
  } else if (isRD && w == 0) {
    const u32 r = xcd * 5 + (j - 18);
    const rsrc_t rO = mk(out, D * 2);
    const u32 xh = ld4<0>(rX, (l & 7) * 4, r * 32);
    float y0, y1;
    for (u32 spin = 0;; ++spin) {
      asm volatile("" ::: "memory");
      const u32x4 g = ld16<SC1>(rWS, (l >> 3) * 5120 + (l & 7) * 16, WS_Y + r * 128);
      y0 = f_(g[0]); y1 = f_(g[2]);
      if (__builtin_amdgcn_ballot_w64(((g[1] ^ tag) | (g[3] ^ tag)) != 0) == 0 || spin > SPIN_CAP) break;
    }
    STAMP(10);
    const u32 at = pk(xoct_add(y0), xoct_add(y1));
    const u32 hp = pk(blo(xh) + blo(at), bhi(xh) + bhi(at));
    const bool tail = l >= 48;
    for (u32 spin = 0;; ++spin) {
      asm volatile("" ::: "memory");
      u32x4 g[7];
#pragma unroll
      for (int i = 0; i < 6; ++i) g[i] = ld16<SC1>(rWS, (l >> 3) * 5120 + (l & 7) * 16, WS_P4 + r * 128 + i * 40960);
      g[6] = ld16<SC1>(rWS, (tail ? 5 : (l >> 3)) * 5120 + (l & 7) * 16, WS_P4 + r * 128 + 6 * 40960);
      u32 bad = 0; y0 = tail ? 0.f : f_(g[6][0]); y1 = tail ? 0.f : f_(g[6][2]);
#pragma unroll
      for (int i = 0; i < 7; ++i) bad |= (g[i][1] ^ tag) | (g[i][3] ^ tag);
#pragma unroll
      for (int i = 0; i < 6; ++i) { y0 += f_(g[i][0]); y1 += f_(g[i][2]); }
      if (__builtin_amdgcn_ballot_w64(bad != 0) == 0 || spin > SPIN_CAP) break;
    }
    STAMP(11);
    const u32 ff = pk(xoct_add(y0), xoct_add(y1));
    const u32 o = pk(blo(hp) + blo(ff), bhi(hp) + bhi(ff));
    if (l < 8) __builtin_amdgcn_raw_buffer_store_b32(o, rO, l * 4, r * 32, SC1);
    if (r == 0 && l == 0) __builtin_amdgcn_raw_buffer_store_b32(tag, rWS, 0, 0, SC1);
    STAMP(12);
  }
  FLUSH_STAMPS();
}

extern "C" void kernel_launch(void* const* d_in, const int* in_sizes, int n_in, void* d_out, int out_size, void* d_ws, size_t ws_size,
                              hipStream_t stream) {
  fused_block<<<256, 512, 0, stream>>>(d_in[0], d_in[1], d_in[2], d_in[3], d_in[4], d_in[6], d_in[7], d_out, d_ws);
}
```

```cpp
#include <hip/hip_runtime.h>
#include <stdint.h>
constexpr unsigned WS_ST = 16777216u; constexpr int NWAVE = 12;
#define ROLE_OF(b) ((((b) >> 3) >= 4 && ((b) >> 3) < 24) ? 1 : (((b) >> 3) >= 24 && ((b) >> 3) < 31) ? 2 : 3)
#define SRD(n) const void* n, unsigned n##_bytes, unsigned n##_fmt
__global__ void __launch_bounds__(768) __attribute__((aligned(4096)))
fused_block(SRD(kc), SRD(vc), SRD(ws), SRD(x), SRD(wq), SRD(wo), SRD(w1), SRD(w2), SRD(out)) {
  __shared__ __attribute__((aligned(16))) char lds[93696];
  asm volatile(
      ".Lk_start:\n\t"
      "s_load_dwordx16 s[4:19], %0, 0x0\n\t"
      "s_load_dwordx16 s[20:35], %0, 0x40\n\t"
      "s_load_dwordx4 s[36:39], %0, 0x80\n\t"
      "s_memrealtime s[40:41]\n\t"
      "v_lshrrev_b32 v2, 6, %2\n\t"
      "v_and_b32 v1, 63, %2\n\t"
      "s_and_b32 s42, %1, 7\n\t"
      "v_readfirstlane_b32 s44, v2\n\t"
      "s_lshr_b32 s43, %1, 3\n\t"
      "s_mov_b32 s47, 0xffffff00\n\t"
      "s_mov_b32 s64, 0\n\t"
      "s_cmp_ge_u32 s43, 24\n\t"
      "s_cbranch_scc1 .Lk_other\n\t"
      "s_and_b32 s48, s43, 3\n\t"
      "s_lshl_b32 s51, s42, 2\n\t"
      "s_add_u32 s48, s48, s51\n\t"
      "s_lshr_b32 s49, s43, 2\n\t"
      "s_mul_i32 s50, s49, 11\n\t"
      "s_min_u32 s50, s50, 54\n\t"
      "s_cmp_eq_u32 s44, 0\n\t"
      "s_cbranch_scc0 .Lk_stream\n\t"
      "v_lshlrev_b32 v2, 4, v1\n\t"
      "v_lshlrev_b32 v3, 2, v1\n\t"
      "s_mul_i32 s51, s50, 20\n\t"
      "s_waitcnt lgkmcnt(0)\n\t"
      "buffer_load_dwordx4 v[4:7], v2, s[16:19], 0 offen\n\t"
      "buffer_load_dword v8, v3, s[16:19], 0 offen offset:1024\n\t"
      "buffer_load_dword v9, v3, s[16:19], s51 offen\n\t"
      "s_load_dword s45, s[12:13], 0x0\n\t"
      "s_branch .Lk_comm\n\t"
      ".Lk_stream:\n\t"
      "v_lshrrev_b32 v48, 4, v1\n\t"
      "v_and_b32 v49, 15, v1\n\t"
      "s_add_u32 s51, s50, s44\n\t"
      "s_sub_u32 s51, s51, 1\n\t"
      "v_lshlrev_b32 v50, 4, v49\n\t"
      "s_mul_i32 s52, s51, 81920\n\t"
      "s_lshl_b32 s53, s48, 8\n\t"
      "v_lshl_or_b32 v2, v48, 14, v50\n\t"
      "s_add_u32 s52, s52, s53\n\t"
      "s_add_u32 s53, s52, 0x2000\n\t"
      "v_and_b32 v3, 1, v48\n\t"
      "s_add_u32 s54, s52, 0x10000\n\t"
      "v_lshl_or_b32 v3, v3, 13, v50\n\t"
      "s_cmp_ge_u32 s49, 4\n\t"
      "s_cselect_b32 s55, 11, 12\n\t"
      "s_cmp_eq_u32 s44, s55\n\t"
      "s_waitcnt lgkmcnt(0)\n\t"
      "s_cselect_b32 s22, 0, s22\n\t"
      "s_cselect_b32 s6, 0, s6\n\t"
      "s_cselect_b32 s10, 0, s10\n\t"
      "s_cselect_b32 s63, 1, 0\n\t"
      "buffer_load_dwordx4 v[4:7], v2, s[20:23], s52 offen nt\n\t"
      "buffer_load_dwordx4 v[8:11], v2, s[20:23], s53 offen nt\n\t"
      "buffer_load_dwordx4 v[12:15], v3, s[20:23], s54 offen nt\n\t"
      "s_load_dword s45, s[12:13], 0x0\n\t"
      "s_lshl_b32 s51, s51, 12\n\t"
      "s_lshl_b32 s52, s48, 20\n\t"
      "s_add_u32 s51, s51, s52\n\t"
      "s_add_u32 s52, s43, 0\n\t"
      "s_and_b32 s52, s52, 3\n\t"
      "s_lshl_b32 s52, s52, 10\n\t"
      "s_add_u32 s56, s51, s52\n\t"
      "s_add_u32 s52, s43, 1\n\t"
      "s_and_b32 s52, s52, 3\n\t"
      "s_lshl_b32 s52, s52, 10\n\t"
      "s_add_u32 s57, s51, s52\n\t"
      "s_add_u32 s52, s43, 2\n\t"
      "s_and_b32 s52, s52, 3\n\t"
      "s_lshl_b32 s52, s52, 10\n\t"
      "s_add_u32 s58, s51, s52\n\t"
      "s_add_u32 s52, s43, 3\n\t"
      "s_and_b32 s52, s52, 3\n\t"
      "s_lshl_b32 s52, s52, 10\n\t"
      "s_add_u32 s59, s51, s52\n\t"
      "v_and_b32 v51, 1, v1\n\t"
      "s_mov_b32 s65, 0xffff\n\t"
      "v_lshlrev_b32 v51, 4, v51\n\t"
      "v_lshlrev_b32_e64 v3, v51, s65\n\t"
      "v_lshlrev_b32 v2, 4, v1\n\t"
      "s_lshl_b32 s51, s44, 9\n\t"
      "s_sub_u32 s51, s51, 512\n\t"
      "v_lshlrev_b32 v51, 3, v48\n\t"
      "v_lshl_or_b32 v85, v49, 5, v51\n\t"
      "v_add_u32 v85, s51, v85\n\t"
      "s_mul_i32 s51, s44, 40\n\t"
      "s_sub_u32 s51, s51, 40\n\t"
      "v_add_u32 v86, s51, v51\n\t"
      "v_and_b32 v51, 1, v48\n\t"
      "v_lshl_add_u32 v87, v51, 2, s51\n\t"
      "s_lshl_b32 s51, s44, 3\n\t"
      "s_sub_u32 s51, s51, 8\n\t"
      "v_mov_b32 v68, s51\n\t"
      "v_mov_b32 v69, 1\n\t"
      "v_mov_b32 v70, 6528\n\t"
      "s_mov_b32 s55, 0x3db504f3\n\t"
      "v_mov_b32 v76, 0\n\t"
      "v_mov_b32 v77, 0\n\t"
      "v_mov_b32 v78, 0\n\t"
      "v_mov_b32 v79, 0\n\t"
      "v_mov_b32 v80, 0\n\t"
      "v_mov_b32 v81, 0\n\t"
      "v_mov_b32 v82, 0\n\t"
      "v_mov_b32 v83, 0\n\t"
      "v_mov_b32 v52, 0\n\t"
      "v_mov_b32 v53, 0\n\t"
      "v_mov_b32 v54, 0\n\t"
      "v_mov_b32 v55, 0\n\t"
      "s_sub_u32 s51, s43, 4\n\t"
      "s_cmp_ge_u32 s51, 10\n\t"
      "s_cselect_b32 s60, 1, 0\n\t"
      "s_cselect_b32 s52, 10, 0\n\t"
      "s_sub_u32 s61, s51, s52\n\t"
      "s_lshl_b32 s52, s42, 1\n\t"
      "s_add_u32 s60, s60, s52\n\t"
      "s_cmp_lt_u32 s51, 20\n\t"
      "s_cselect_b32 s26, s26, 0\n\t"
      "s_cmp_lt_u32 s44, 9\n\t"
      "s_cselect_b32 s26, s26, 0\n\t"
      "s_lshl_b32 s52, s60, 3\n\t"
      "s_add_u32 s52, s52, s44\n\t"
      "s_sub_u32 s62, s52, 1\n\t"
      "s_mul_i32 s52, s62, 40960\n\t"
      "s_lshl_b32 s53, s61, 7\n\t"
      "s_add_u32 s66, s52, s53\n\t"
      "s_add_u32 s67, s66, 5120\n\t"
      "s_add_u32 s68, s66, 20480\n\t"
      "s_add_u32 s69, s66, 25600\n\t"
      "v_bfe_u32 v71, v1, 3, 1\n\t"
      "v_lshl_or_b32 v71, v71, 3, v48\n\t"
      "v_mul_u32_u24 v71, 1280, v71\n\t"
      "v_and_b32 v51, 7, v1\n\t"
      "v_lshl_or_b32 v71, v51, 4, v71\n\t"
      "s_waitcnt lgkmcnt(0)\n\t"
      "s_barrier\n\t"
      "s_add_u32 s45, s45, 1\n\t"
      "s_and_b32 s46, s45, 0xff\n\t"
      "ds_read_b64 v[60:61], v86 offset:8160\n\t"
      "ds_read_b32 v62, v87 offset:8192\n\t"
      "s_waitcnt vmcnt(2)\n\t"
      "buffer_load_dwordx4 v[16:19], v2, s[4:7], s56 offen nt\n\t"
      "s_waitcnt lgkmcnt(0)\n\t"
      "v_and_b32_dpp v76, v60, v3 quad_perm:[0,1,2,3] row_mask:0xf bank_mask:0x1\n\t"
      "v_and_b32_dpp v77, v60, v3 quad_perm:[0,1,2,3] row_mask:0xf bank_mask:0x2\n\t"
      "v_and_b32_dpp v78, v60, v3 quad_perm:[0,1,2,3] row_mask:0xf bank_mask:0x4\n\t"
      "v_and_b32_dpp v79, v60, v3 quad_perm:[0,1,2,3] row_mask:0xf bank_mask:0x8\n\t"
      "v_and_b32_dpp v80, v61, v3 quad_perm:[0,1,2,3] row_mask:0xf bank_mask:0x1\n\t"
      "v_and_b32_dpp v81, v61, v3 quad_perm:[0,1,2,3] row_mask:0xf bank_mask:0x2\n\t"
      "v_and_b32_dpp v82, v61, v3 quad_perm:[0,1,2,3] row_mask:0xf bank_mask:0x4\n\t"
      "v_and_b32_dpp v83, v61, v3 quad_perm:[0,1,2,3] row_mask:0xf bank_mask:0x8\n\t"
      "v_and_b32_dpp v52, v62, v3 quad_perm:[0,1,2,3] row_mask:0x3 bank_mask:0x1\n\t"
      "v_and_b32_dpp v53, v62, v3 quad_perm:[0,1,2,3] row_mask:0x3 bank_mask:0x2\n\t"
      "v_and_b32_dpp v54, v62, v3 quad_perm:[0,1,2,3] row_mask:0x3 bank_mask:0x4\n\t"
      "v_and_b32_dpp v55, v62, v3 quad_perm:[0,1,2,3] row_mask:0x3 bank_mask:0x8\n\t"
      "v_mfma_f32_16x16x32_bf16 v[96:99], v[76:79], v[4:7], 0\n\t"
      "s_waitcnt vmcnt(2)\n\t"
      "buffer_load_dwordx4 v[20:23], v2, s[4:7], s57 offen nt\n\t"
      "v_mfma_f32_16x16x32_bf16 v[96:99], v[80:83], v[8:11], v[96:99]\n\t"
      "s_waitcnt vmcnt(2)\n\t"
      "buffer_load_dwordx4 v[24:27], v2, s[4:7], s58 offen nt\n\t"
      "v_mfma_f32_16x16x32_bf16 v[96:99], v[52:55], v[12:15], v[96:99]\n\t"
      "s_nop 7\n\t"
      "ds_write_b64 v85, v[96:97] offset:0\n\t"
      "s_mov_b64 exec, 1\n\t"
      "ds_add_u32 v70, v69\n\t"
      "s_mov_b64 exec, -1\n\t"
      "s_waitcnt vmcnt(2)\n\t"
      "buffer_load_dwordx4 v[28:31], v2, s[4:7], s59 offen nt\n\t"
      "s_waitcnt vmcnt(2)\n\t"
      "buffer_load_dwordx4 v[32:35], v2, s[8:11], s56 offen nt\n\t"
      "s_waitcnt vmcnt(2)\n\t"
      "buffer_load_dwordx4 v[36:39], v2, s[8:11], s57 offen nt\n\t"
      "s_waitcnt vmcnt(2)\n\t"
      "buffer_load_dwordx4 v[40:43], v2, s[8:11], s58 offen nt\n\t"
      "s_waitcnt vmcnt(2)\n\t"
      "buffer_load_dwordx4 v[44:47], v2, s[8:11], s59 offen nt\n\t"
      "s_waitcnt lgkmcnt(0)\n\t"
      "s_barrier\n\t"
      "ds_read_b128 v[72:75], v50 offset:6144\n\t"
      "s_waitcnt lgkmcnt(0)\n\t"
      "v_dot2_f32_bf16 v88, v16, v72, 0\n\t"
      "v_dot2_f32_bf16 v89, v20, v72, 0\n\t"
      "v_dot2_f32_bf16 v90, v24, v72, 0\n\t"
      "v_dot2_f32_bf16 v91, v28, v72, 0\n\t"
      "v_dot2c_f32_bf16 v88, v17, v73\n\t"
      "v_dot2c_f32_bf16 v89, v21, v73\n\t"
      "v_dot2c_f32_bf16 v90, v25, v73\n\t"
      "v_dot2c_f32_bf16 v91, v29, v73\n\t"
      "v_dot2c_f32_bf16 v88, v18, v74\n\t"
      "v_dot2c_f32_bf16 v89, v22, v74\n\t"
      "v_dot2c_f32_bf16 v90, v26, v74\n\t"
      "v_dot2c_f32_bf16 v91, v30, v74\n\t"
      "v_dot2c_f32_bf16 v88, v19, v75\n\t"
      "v_dot2c_f32_bf16 v89, v23, v75\n\t"
      "v_dot2c_f32_bf16 v90, v27, v75\n\t"
      "v_dot2c_f32_bf16 v91, v31, v75\n\t"
      "v_add_f32_dpp v88, v88, v88 row_ror:8 row_mask:0xf bank_mask:0xf\n\t"
      "v_add_f32_dpp v89, v89, v89 row_ror:8 row_mask:0xf bank_mask:0xf\n\t"
      "s_nop 0\n\t"
      "v_add_f32_dpp v88, v90, v90 row_ror:8 row_mask:0xf bank_mask:0xc\n\t"
      "v_add_f32_dpp v89, v91, v91 row_ror:8 row_mask:0xf bank_mask:0xc\n\t"
      "s_nop 0\n\t"
      "v_add_f32_dpp v88, v88, v88 row_half_mirror row_mask:0xf bank_mask:0xf\n\t"
      "s_nop 1\n\t"
      "v_add_f32_dpp v88, v89, v89 row_half_mirror row_mask:0xf bank_mask:0xa\n\t"
      "s_nop 1\n\t"
      "v_add_f32_dpp v88, v88, v88 quad_perm:[1,0,3,2] row_mask:0xf bank_mask:0xf\n\t"
      "s_nop 1\n\t"
      "v_add_f32_dpp v88, v88, v88 quad_perm:[2,3,0,1] row_mask:0xf bank_mask:0xf\n\t"
      "v_cvt_pk_bf16_f32 v88, v88, v88\n\t"
      "v_lshlrev_b32 v88, 16, v88\n\t"
      "v_mul_f32 v88, s55, v88\n\t"
      "v_cvt_pk_bf16_f32 v88, v88, v88\n\t"
      "v_lshlrev_b32 v88, 16, v88\n\t"
      "s_nop 1\n\t"
      "v_max_f32_dpp v90, v88, v88 row_ror:4 row_mask:0xf bank_mask:0xf\n\t"
      "s_nop 1\n\t"
      "v_max_f32_dpp v90, v90, v90 row_ror:8 row_mask:0xf bank_mask:0xf\n\t"
      "v_mov_b32 v91, v90\n\t"
      "s_nop 1\n\t"
      "v_permlane16_swap_b32 v90, v91\n\t"
      "v_max_f32 v90, v90, v91\n\t"
      "v_mov_b32 v91, v90\n\t"
      "s_nop 1\n\t"
      "v_permlane32_swap_b32 v90, v91\n\t"
      "v_max_f32 v90, v90, v91\n\t"
      "v_sub_f32 v88, v88, v90\n\t"
      "v_mul_f32 v88, 0x3fb8aa3b, v88\n\t"
      "v_exp_f32 v91, v88\n\t"
      "s_nop 0\n\t"
      "v_cvt_pk_bf16_f32 v92, v91, v91\n\t"
      "s_nop 1\n\t"
      "v_and_b32_dpp v76, v92, v3 row_newbcast:0 row_mask:0xf bank_mask:0x1\n\t"
      "v_and_b32_dpp v77, v92, v3 row_newbcast:0 row_mask:0xf bank_mask:0x2\n\t"
      "v_and_b32_dpp v78, v92, v3 row_newbcast:0 row_mask:0xf bank_mask:0x4\n\t"
      "v_and_b32_dpp v79, v92, v3 row_newbcast:0 row_mask:0xf bank_mask:0x8\n\t"
      "v_add_f32_dpp v91, v91, v91 row_ror:4 row_mask:0xf bank_mask:0xf\n\t"
      "s_waitcnt vmcnt(3)\n\t"
      "v_mfma_f32_16x16x32_bf16 v[96:99], v[76:79], v[32:35], 0\n\t"
      "buffer_load_dwordx4 v[100:103], v71, s[24:27], s66 offen nt\n\t"
      "v_and_b32_dpp v80, v92, v3 row_newbcast:4 row_mask:0xf bank_mask:0x1\n\t"
      "v_and_b32_dpp v81, v92, v3 row_newbcast:4 row_mask:0xf bank_mask:0x2\n\t"
      "v_and_b32_dpp v82, v92, v3 row_newbcast:4 row_mask:0xf bank_mask:0x4\n\t"
      "v_and_b32_dpp v83, v92, v3 row_newbcast:4 row_mask:0xf bank_mask:0x8\n\t"
      "v_add_f32_dpp v91, v91, v91 row_ror:8 row_mask:0xf bank_mask:0xf\n\t"
      "v_mov_b32 v94, v91\n\t"
      "s_waitcnt vmcnt(3)\n\t"
      "v_mfma_f32_16x16x32_bf16 v[96:99], v[80:83], v[36:39], v[96:99]\n\t"
      "buffer_load_dwordx4 v[104:107], v71, s[24:27], s67 offen nt\n\t"
      "v_and_b32_dpp v76, v92, v3 row_newbcast:8 row_mask:0xf bank_mask:0x1\n\t"
      "v_and_b32_dpp v77, v92, v3 row_newbcast:8 row_mask:0xf bank_mask:0x2\n\t"
      "v_and_b32_dpp v78, v92, v3 row_newbcast:8 row_mask:0xf bank_mask:0x4\n\t"
      "v_and_b32_dpp v79, v92, v3 row_newbcast:8 row_mask:0xf bank_mask:0x8\n\t"
      "v_permlane16_swap_b32 v91, v94\n\t"
      "v_add_f32 v91, v91, v94\n\t"
      "v_mov_b32 v94, v91\n\t"
      "s_waitcnt vmcnt(3)\n\t"
      "v_mfma_f32_16x16x32_bf16 v[96:99], v[76:79], v[40:43], v[96:99]\n\t"
      "v_and_b32_dpp v80, v92, v3 row_newbcast:12 row_mask:0xf bank_mask:0x1\n\t"
      "v_and_b32_dpp v81, v92, v3 row_newbcast:12 row_mask:0xf bank_mask:0x2\n\t"
      "v_and_b32_dpp v82, v92, v3 row_newbcast:12 row_mask:0xf bank_mask:0x4\n\t"
      "v_and_b32_dpp v83, v92, v3 row_newbcast:12 row_mask:0xf bank_mask:0x8\n\t"
      "v_permlane32_swap_b32 v91, v94\n\t"
      "v_add_f32 v91, v91, v94\n\t"
      "s_waitcnt vmcnt(2)\n\t"
      "v_mfma_f32_16x16x32_bf16 v[96:99], v[80:83], v[44:47], v[96:99]\n\t"
      "s_cmp_eq_u32 s63, 0\n\t"
      "s_cbranch_scc1 .Lk_live\n\t"
      "v_mov_b32 v90, 0xff800000\n\t"
      ".Lk_live:\n\t"
      "s_mov_b64 exec, 1\n\t"
      "ds_write_b64 v68, v[90:91] offset:6400\n\t"
      "s_mov_b64 exec, -1\n\t"
      "s_nop 1\n\t"
      "ds_write_b64 v85, v[96:97] offset:0\n\t"
      "s_waitcnt lgkmcnt(0)\n\t"
      "s_barrier\n\t"
      "s_cmp_lg_u32 s26, 0\n\t"
      "s_cbranch_scc1 .Lk_wo\n\t"
      ".Lk_end:\n\t"
      "s_endpgm\n\t"
      ".Lk_wo:\n\t"
      "v_lshrrev_b32 v40, 3, v1\n\t"
      "v_and_b32 v41, 7, v1\n\t"
      "v_xor_b32 v42, v40, v41\n\t"
      "s_barrier\n\t"
      "buffer_load_dwordx4 v[108:111], v71, s[24:27], s68 offen nt\n\t"
      "buffer_load_dwordx4 v[112:115], v71, s[24:27], s69 offen nt\n\t"
      "v_cmp_lt_u32 s[52:53], 5, v42\n\t"
      "v_min_u32 v42, 5, v42\n\t"
      "v_min_u32 v40, 5, v40\n\t"
      "v_mov_b32 v36, 0xff800000\n\t"
      "v_lshlrev_b32 v37, 3, v42\n\t"
      "v_lshlrev_b32 v38, 4, v41\n\t"
      "v_lshl_or_b32 v38, v40, 9, v38\n\t"
      "s_lshr_b32 s54, s62, 2\n\t"
      "s_lshl_b32 s56, s54, 6\n\t"
      "s_add_u32 s56, s56, 266240\n\t"
      "s_lshl_b32 s54, s54, 12\n\t"
      "s_and_b32 s57, s62, 3\n\t"
      "s_lshl_b32 s57, s57, 7\n\t"
      "s_add_u32 s57, s57, s54\n\t"
      "s_add_u32 s57, s57, 135168\n\t"
      "v_lshlrev_b32 v39, 3, v48\n\t"
      "v_lshl_or_b32 v39, v41, 5, v39\n\t"
      "s_lshl_b32 s54, s44, 8\n\t"
      "s_sub_u32 s54, s54, 256\n\t"
      "v_add_u32 v39, s54, v39\n\t"
      "s_mov_b32 s58, 0x00ff00ff\n\t"
      "s_mov_b32 s59, 0x00ff00ff\n\t"
      "s_waitcnt vmcnt(0)\n\t"
      ".Lk_wop:\n\t"
      "buffer_load_dwordx2 v[40:41], v37, s[12:15], s56 offen sc1\n\t"
      "buffer_load_dwordx4 v[44:47], v38, s[12:15], s57 offen sc1\n\t"
      "s_waitcnt vmcnt(1)\n\t"
      "v_and_b32 v42, s47, v40\n\t"
      "v_and_b32 v43, s47, v41\n\t"
      "v_cndmask_b32_e64 v42, v42, v36, s[52:53]\n\t"
      "v_xor_b32 v56, s46, v40\n\t"
      "v_bitop3_b32 v56, v56, v41, s46 bitop3:0xf6\n\t"
      "v_max_f32_dpp v49, v42, v42 quad_perm:[1,0,3,2] row_mask:0xf bank_mask:0xf\n\t"
      "s_waitcnt vmcnt(0)\n\t"
      "v_bitop3_b32 v56, v56, v44, s46 bitop3:0xf6\n\t"
      "v_bitop3_b32 v56, v56, v45, s46 bitop3:0xf6\n\t"
      "v_max_f32_dpp v49, v49, v49 quad_perm:[2,3,0,1] row_mask:0xf bank_mask:0xf\n\t"
      "v_bitop3_b32 v56, v56, v46, s46 bitop3:0xf6\n\t"
      "v_bitop3_b32 v56, v56, v47, s46 bitop3:0xf6\n\t"
      "v_max_f32_dpp v49, v49, v49 row_half_mirror row_mask:0xf bank_mask:0xf\n\t"
      "v_cmp_ne_u32_sdwa vcc, v56, 0 src0_sel:BYTE_0 src1_sel:DWORD\n\t"
      "v_sub_f32 v42, v42, v49\n\t"
      "v_mul_f32 v42, 0x3fb8aa3b, v42\n\t"
      "s_cbranch_vccnz .Lk_wobad\n\t"
      ".Lk_wook:\n\t"
      "v_exp_f32 v42, v42\n\t"
      "v_and_b32 v44, s47, v44\n\t"
      "v_mul_f32 v43, v42, v43\n\t"
      "v_and_b32 v45, s47, v45\n\t"
      "v_mov_b32_dpp v49, v42 row_newbcast:0 row_mask:0xf bank_mask:0x3\n\t"
      "v_add_f32_dpp v43, v43, v43 quad_perm:[1,0,3,2] row_mask:0xf bank_mask:0xf\n\t"
      "s_nop 0\n\t"
      "v_mov_b32_dpp v49, v42 row_newbcast:8 row_mask:0xf bank_mask:0xc\n\t"
      "v_and_b32 v46, s47, v46\n\t"
      "v_add_f32_dpp v43, v43, v43 quad_perm:[2,3,0,1] row_mask:0xf bank_mask:0xf\n\t"
      "v_and_b32 v47, s47, v47\n\t"
      "v_pk_mul_f32 v[44:45], v[44:45], v[48:49] op_sel:[0,1] op_sel_hi:[1,1]\n\t"
      "v_add_f32_dpp v43, v43, v43 row_half_mirror row_mask:0xf bank_mask:0xf\n\t"
      "v_pk_mul_f32 v[46:47], v[46:47], v[48:49] op_sel:[0,1] op_sel_hi:[1,1]\n\t"
      "v_rcp_f32 v43, v43\n\t"
      "s_nop 0\n\t"
      "v_permlane32_swap_b32 v44, v46\n\t"
      "v_permlane32_swap_b32 v45, v47\n\t"
      "v_pk_add_f32 v[44:45], v[44:45], v[46:47]\n\t"
      "s_nop 1\n\t"
      "v_permlane16_swap_b32 v44, v45\n\t"
      "v_add_f32 v44, v44, v45\n\t"
      "s_nop 1\n\t"
      "v_add_f32_dpp v44, v44, v44 row_ror:8 row_mask:0xf bank_mask:0xf\n\t"
      "v_mul_f32 v44, v44, v43\n\t"
      "v_cvt_pk_bf16_f32 v44, v44, v44\n\t"
      "s_nop 1\n\t"
      "v_and_b32_dpp v50, v44, v3 quad_perm:[0,0,2,2] row_mask:0xf bank_mask:0xf\n\t"
      "v_and_b32_dpp v51, v44, v3 quad_perm:[1,1,3,3] row_mask:0xf bank_mask:0xf\n\t"
      "s_nop 0\n\t"
      "v_mov_b32_dpp v76, v50 quad_perm:[0,1,2,3] row_mask:0xf bank_mask:0x1\n\t"
      "v_mov_b32_dpp v77, v50 row_ror:4 row_mask:0xf bank_mask:0x2\n\t"
      "v_mov_b32_dpp v78, v50 row_ror:8 row_mask:0xf bank_mask:0x4\n\t"
      "v_mov_b32_dpp v79, v50 row_ror:12 row_mask:0xf bank_mask:0x8\n\t"
      "s_nop 1\n\t"
      "v_mfma_f32_16x16x32_bf16 v[64:67], v[76:79], v[100:103], 0\n\t"
      "v_mov_b32_dpp v80, v51 quad_perm:[0,1,2,3] row_mask:0xf bank_mask:0x1\n\t"
      "v_mov_b32_dpp v81, v51 row_ror:4 row_mask:0xf bank_mask:0x2\n\t"
      "v_mov_b32_dpp v82, v51 row_ror:8 row_mask:0xf bank_mask:0x4\n\t"
      "v_mov_b32_dpp v83, v51 row_ror:12 row_mask:0xf bank_mask:0x8\n\t"
      "s_nop 1\n\t"
      "v_mfma_f32_16x16x32_bf16 v[64:67], v[80:83], v[104:107], v[64:67]\n\t"
      "v_mov_b32_dpp v76, v50 row_ror:12 row_mask:0xf bank_mask:0x1\n\t"
      "v_mov_b32_dpp v77, v50 quad_perm:[0,1,2,3] row_mask:0xf bank_mask:0x2\n\t"
      "v_mov_b32_dpp v78, v50 row_ror:4 row_mask:0xf bank_mask:0x4\n\t"
      "v_mov_b32_dpp v79, v50 row_ror:8 row_mask:0xf bank_mask:0x8\n\t"
      "s_nop 1\n\t"
      "v_mfma_f32_16x16x32_bf16 v[64:67], v[76:79], v[108:111], v[64:67]\n\t"
      "v_mov_b32_dpp v80, v51 row_ror:12 row_mask:0xf bank_mask:0x1\n\t"
      "v_mov_b32_dpp v81, v51 quad_perm:[0,1,2,3] row_mask:0xf bank_mask:0x2\n\t"
      "v_mov_b32_dpp v82, v51 row_ror:4 row_mask:0xf bank_mask:0x4\n\t"
      "v_mov_b32_dpp v83, v51 row_ror:8 row_mask:0xf bank_mask:0x8\n\t"
      "s_nop 1\n\t"
      "v_mfma_f32_16x16x32_bf16 v[64:67], v[80:83], v[112:115], v[64:67]\n\t"
      "s_nop 7\n\t"
      "v_add_f32_dpp v64, v66, v64 row_ror:8 row_mask:0xf bank_mask:0xf\n\t"
      "v_add_f32_dpp v65, v67, v65 row_ror:8 row_mask:0xf bank_mask:0xf\n\t"
      "s_mov_b64 exec, s[58:59]\n\t"
      "ds_write_b64 v39, v[64:65] offset:0\n\t"
      "s_mov_b64 exec, -1\n\t"
      "s_waitcnt lgkmcnt(0)\n\t"
      "s_barrier\n\t"
      "s_endpgm\n\t"
      ".Lk_wobad:\n\t"
      "s_sleep 1\n\t"
      "s_add_u32 s64, s64, 1\n\t"
      "s_cmp_lt_u32 s64, 262144\n\t"
      "s_cbranch_scc1 .Lk_wop\n\t"
      "s_branch .Lk_wook\n\t"
      ".Lk_comm:\n\t"
      "s_setprio 3\n\t"
      "v_lshlrev_b32 v31, 3, v1\n\t"
      "v_mov_b32 v32, 0\n\t"
      "v_mov_b32 v33, 0\n\t"
      "v_mov_b32 v34, 0xff800000\n\t"
      "v_mov_b32 v35, 0\n\t"
      "v_mov_b32 v70, 6528\n\t"
      "ds_write_b32 v70, v32\n\t"
      "ds_write_b64 v31, v[32:33] offset:5632\n\t"
      "s_mov_b64 exec, 0x1f\n\t"
      "ds_write_b64 v31, v[34:35] offset:6488\n\t"
      "s_mov_b64 exec, -1\n\t"
      "s_waitcnt vmcnt(1)\n\t"
      "v_lshlrev_b32 v10, 16, v4\n\t"
      "v_and_b32 v11, 0xffff0000, v4\n\t"
      "v_lshlrev_b32 v12, 16, v5\n\t"
      "v_and_b32 v13, 0xffff0000, v5\n\t"
      "v_lshlrev_b32 v14, 16, v6\n\t"
      "v_and_b32 v15, 0xffff0000, v6\n\t"
      "v_lshlrev_b32 v16, 16, v7\n\t"
      "v_and_b32 v17, 0xffff0000, v7\n\t"
      "v_lshlrev_b32 v18, 16, v8\n\t"
      "v_and_b32 v19, 0xffff0000, v8\n\t"
      "v_pk_add_f32 v[20:21], v[10:11], v[12:13]\n\t"
      "v_pk_mul_f32 v[22:23], v[10:11], v[10:11]\n\t"
      "v_pk_add_f32 v[24:25], v[14:15], v[16:17]\n\t"
      "v_pk_fma_f32 v[22:23], v[12:13], v[12:13], v[22:23]\n\t"
      "v_pk_add_f32 v[20:21], v[20:21], v[18:19]\n\t"
      "v_pk_fma_f32 v[22:23], v[14:15], v[14:15], v[22:23]\n\t"
      "v_pk_add_f32 v[20:21], v[20:21], v[24:25]\n\t"
      "v_pk_fma_f32 v[22:23], v[16:17], v[16:17], v[22:23]\n\t"
      "v_add_f32 v20, v20, v21\n\t"
      "v_pk_fma_f32 v[22:23], v[18:19], v[18:19], v[22:23]\n\t"
      "v_add_f32 v22, v22, v23\n\t"
      "v_add_f32_dpp v20, v20, v20 quad_perm:[1,0,3,2] row_mask:0xf bank_mask:0xf\n\t"
      "s_nop 0\n\t"
      "v_add_f32_dpp v22, v22, v22 quad_perm:[1,0,3,2] row_mask:0xf bank_mask:0xf\n\t"
      "v_add_f32_dpp v20, v20, v20 quad_perm:[2,3,0,1] row_mask:0xf bank_mask:0xf\n\t"
      "s_nop 0\n\t"
      "v_add_f32_dpp v22, v22, v22 quad_perm:[2,3,0,1] row_mask:0xf bank_mask:0xf\n\t"
      "v_add_f32_dpp v20, v20, v20 row_half_mirror row_mask:0xf bank_mask:0xf\n\t"
      "s_nop 0\n\t"
      "v_add_f32_dpp v22, v22, v22 row_half_mirror row_mask:0xf bank_mask:0xf\n\t"
      "v_add_f32_dpp v20, v20, v20 row_ror:8 row_mask:0xf bank_mask:0xf\n\t"
      "s_nop 0\n\t"
      "v_add_f32_dpp v22, v22, v22 row_ror:8 row_mask:0xf bank_mask:0xf\n\t"
      "s_nop 1\n\t"
      "v_permlane16_swap_b32 v20, v22\n\t"
      "v_add_f32 v20, v20, v22\n\t"
      "v_mov_b32 v21, v20\n\t"
      "s_nop 1\n\t"
      "v_permlane32_swap_b32 v20, v21\n\t"
      "v_add_f32 v20, v20, v21\n\t"
      "v_mov_b32 v22, v20\n\t"
      "s_nop 1\n\t"
      "v_permlane16_swap_b32 v20, v22\n\t"
      "s_waitcnt vmcnt(0)\n\t"
      "v_lshlrev_b32 v10, 16, v9\n\t"
      "v_and_b32 v11, 0xffff0000, v9\n\t"
      "v_mul_f32 v20, 0x3acccccd, v20\n\t"
      "v_mul_f32 v22, 0x3acccccd, v22\n\t"
      "v_fma_f32 v22, -v20, v20, v22\n\t"
      "v_cvt_pk_bf16_f32 v20, v20, v20\n\t"
      "v_max_f32 v22, 0, v22\n\t"
      "v_lshlrev_b32 v20, 16, v20\n\t"
      "v_cvt_pk_bf16_f32 v22, v22, v22\n\t"
      "v_lshlrev_b32 v22, 16, v22\n\t"
      "v_add_f32 v22, 0x3727c5ac, v22\n\t"
      "v_cvt_pk_bf16_f32 v22, v22, v22\n\t"
      "v_lshlrev_b32 v22, 16, v22\n\t"
      "v_sqrt_f32 v22, v22\n\t"
      "s_nop 0\n\t"
      "v_cvt_pk_bf16_f32 v22, v22, v22\n\t"
      "v_lshlrev_b32 v22, 16, v22\n\t"
      "v_rcp_f32 v22, v22\n\t"
      "v_pk_add_f32 v[10:11], v[10:11], v[20:21] op_sel_hi:[1,0] neg_lo:[0,1] neg_hi:[0,1]\n\t"
      "v_cvt_pk_bf16_f32 v4, v10, v11\n\t"
      "v_lshlrev_b32 v10, 16, v4\n\t"
      "v_and_b32 v11, 0xffff0000, v4\n\t"
      "v_pk_mul_f32 v[10:11], v[10:11], v[22:23] op_sel_hi:[1,0]\n\t"
      "v_cvt_pk_bf16_f32 v10, v10, v10\n\t"
      "v_cvt_pk_bf16_f32 v11, v11, v11\n\t"
      "ds_write_b64 v31, v[10:11] offset:8160\n\t"
      "s_waitcnt lgkmcnt(0)\n\t"
      "s_barrier\n\t"
      "s_add_u32 s45, s45, 1\n\t"
      "s_and_b32 s46, s45, 0xff\n\t"
      "v_mov_b32 v9, s46\n\t"
      "v_and_b32 v4, 31, v1\n\t"
      "v_lshrrev_b32 v5, 5, v1\n\t"
      "v_lshlrev_b32 v6, 2, v5\n\t"
      "v_lshl_or_b32 v6, v4, 3, v6\n\t"
      "v_mul_u32_u24 v7, 3072, v5\n\t"
      "v_lshl_or_b32 v7, v4, 4, v7\n\t"
      "v_lshlrev_b32 v8, 3, v5\n\t"
      "v_lshl_or_b32 v8, v4, 4, v8\n\t"
      "v_and_b32 v4, 15, v1\n\t"
      "v_mul_u32_u24 v5, 6, v5\n\t"
      "v_add_u32 v5, v4, v5\n\t"
      "v_subrev_u32 v10, 12, v5\n\t"
      "v_min_u32 v5, v5, v10\n\t"
      "v_cmp_gt_u32 vcc, 12, v4\n\t"
      "s_nop 1\n\t"
      "v_cndmask_b32 v5, v4, v5, vcc\n\t"
      "v_lshlrev_b32 v5, 3, v5\n\t"
      "s_lshl_b32 s51, s48, 12\n\t"
      "s_lshl_b32 s52, s49, 9\n\t"
      "s_add_u32 s52, s52, s51\n\t"
      "s_add_u32 s53, s52, 4096\n\t"
      "s_add_u32 s52, s52, 135168\n\t"
      "s_add_u32 s51, s51, 4096\n\t"
      "s_add_u32 s54, s12, s51\n\t"
      "s_addc_u32 s55, s13, 0\n\t"
      ".Lk_cnt:\n\t"
      "ds_read_b32 v10, v70\n\t"
      "s_waitcnt lgkmcnt(0)\n\t"
      "v_cmp_ne_u32 vcc, 11, v10\n\t"
      "s_cbranch_vccnz .Lk_cnt\n\t"
      "ds_read_b128 v[32:35], v7 offset:0\n\t"
      "ds_read_b128 v[36:39], v7 offset:512\n\t"
      "ds_read_b128 v[40:43], v7 offset:1024\n\t"
      "ds_read_b128 v[44:47], v7 offset:1536\n\t"
      "ds_read_b128 v[48:51], v7 offset:2048\n\t"
      "ds_read_b128 v[52:55], v7 offset:2560\n\t"
      "s_waitcnt lgkmcnt(4)\n\t"
      "v_pk_add_f32 v[32:33], v[32:33], v[36:37]\n\t"
      "v_pk_add_f32 v[34:35], v[34:35], v[38:39]\n\t"
      "s_waitcnt lgkmcnt(3)\n\t"
      "v_pk_add_f32 v[32:33], v[32:33], v[40:41]\n\t"
      "v_pk_add_f32 v[34:35], v[34:35], v[42:43]\n\t"
      "s_waitcnt lgkmcnt(2)\n\t"
      "v_pk_add_f32 v[32:33], v[32:33], v[44:45]\n\t"
      "v_pk_add_f32 v[34:35], v[34:35], v[46:47]\n\t"
      "s_waitcnt lgkmcnt(1)\n\t"
      "v_pk_add_f32 v[32:33], v[32:33], v[48:49]\n\t"
      "v_pk_add_f32 v[34:35], v[34:35], v[50:51]\n\t"
      "s_waitcnt lgkmcnt(0)\n\t"
      "v_pk_add_f32 v[32:33], v[32:33], v[52:53]\n\t"
      "v_pk_add_f32 v[34:35], v[34:35], v[54:55]\n\t"
      "s_nop 1\n\t"
      "v_permlane32_swap_b32 v32, v34\n\t"
      "v_permlane32_swap_b32 v33, v35\n\t"
      "v_pk_add_f32 v[32:33], v[32:33], v[34:35]\n\t"
      "v_and_or_b32 v32, v32, s47, v9\n\t"
      "v_and_or_b32 v33, v33, s47, v9\n\t"
      "buffer_store_dwordx2 v[32:33], v8, s[12:15], s53 offen sc1\n\t"
      "s_sleep 4\n\t"
      ".Lk_qs:\n\t"
      "s_load_dword s56, s[54:55], 0x1fc glc\n\t"
      "s_load_dword s57, s[54:55], 0x3fc glc\n\t"
      "s_load_dword s58, s[54:55], 0x5fc glc\n\t"
      "s_load_dword s59, s[54:55], 0x7fc glc\n\t"
      "s_load_dword s60, s[54:55], 0x9fc glc\n\t"
      "s_load_dword s61, s[54:55], 0xbfc glc\n\t"
      "s_add_u32 s64, s64, 1\n\t"
      "s_waitcnt lgkmcnt(0)\n\t"
      "s_xor_b32 s65, s56, s46\n\t"
      "s_xor_b32 s57, s57, s46\n\t"
      "s_or_b32 s65, s65, s57\n\t"
      "s_xor_b32 s58, s58, s46\n\t"
      "s_or_b32 s65, s65, s58\n\t"
      "s_xor_b32 s59, s59, s46\n\t"
      "s_or_b32 s65, s65, s59\n\t"
      "s_xor_b32 s60, s60, s46\n\t"
      "s_or_b32 s65, s65, s60\n\t"
      "s_xor_b32 s61, s61, s46\n\t"
      "s_or_b32 s65, s65, s61\n\t"
      "s_and_b32 s65, s65, 0xff\n\t"
      "s_cbranch_scc0 .Lk_qsok\n\t"
      "s_sleep 1\n\t"
      "s_cmp_lt_u32 s64, 4096\n\t"
      "s_cbranch_scc1 .Lk_qs\n\t"
      ".Lk_qsok:\n\t"
      ".Lk_qpoll:\n\t"
      "buffer_load_dwordx4 v[12:15], v2, s[12:15], s51 offen offset:0 sc1\n\t"
      "buffer_load_dwordx4 v[16:19], v2, s[12:15], s51 offen offset:1024 sc1\n\t"
      "buffer_load_dwordx4 v[20:23], v2, s[12:15], s51 offen offset:2048 sc1\n\t"
      "s_waitcnt vmcnt(2)\n\t"
      "v_xor_b32 v10, s46, v12\n\t"
      "v_bitop3_b32 v10, v10, v13, s46 bitop3:0xf6\n\t"
      "v_bitop3_b32 v10, v10, v14, s46 bitop3:0xf6\n\t"
      "v_bitop3_b32 v10, v10, v15, s46 bitop3:0xf6\n\t"
      "v_and_b32 v12, s47, v12\n\t"
      "v_and_b32 v13, s47, v13\n\t"
      "v_and_b32 v14, s47, v14\n\t"
      "v_and_b32 v15, s47, v15\n\t"
      "s_waitcnt vmcnt(1)\n\t"
      "v_bitop3_b32 v10, v10, v16, s46 bitop3:0xf6\n\t"
      "v_bitop3_b32 v10, v10, v17, s46 bitop3:0xf6\n\t"
      "v_bitop3_b32 v10, v10, v18, s46 bitop3:0xf6\n\t"
      "v_bitop3_b32 v10, v10, v19, s46 bitop3:0xf6\n\t"
      "v_and_b32 v16, s47, v16\n\t"
      "v_and_b32 v17, s47, v17\n\t"
      "v_and_b32 v18, s47, v18\n\t"
      "v_and_b32 v19, s47, v19\n\t"
      "v_pk_add_f32 v[12:13], v[12:13], v[16:17]\n\t"
      "v_pk_add_f32 v[14:15], v[14:15], v[18:19]\n\t"
      "s_waitcnt vmcnt(0)\n\t"
      "v_bitop3_b32 v10, v10, v20, s46 bitop3:0xf6\n\t"
      "v_bitop3_b32 v10, v10, v21, s46 bitop3:0xf6\n\t"
      "v_bitop3_b32 v10, v10, v22, s46 bitop3:0xf6\n\t"
      "v_bitop3_b32 v10, v10, v23, s46 bitop3:0xf6\n\t"
      "v_and_b32 v20, s47, v20\n\t"
      "v_and_b32 v21, s47, v21\n\t"
      "v_and_b32 v22, s47, v22\n\t"
      "v_and_b32 v23, s47, v23\n\t"
      "v_pk_add_f32 v[12:13], v[12:13], v[20:21]\n\t"
      "v_pk_add_f32 v[14:15], v[14:15], v[22:23]\n\t"
      "v_cmp_ne_u32_sdwa vcc, v10, 0 src0_sel:BYTE_0 src1_sel:DWORD\n\t"
      "s_cbranch_vccnz .Lk_qbad\n\t"
      ".Lk_qok:\n\t"
      "v_permlane32_swap_b32 v12, v14\n\t"
      "v_permlane32_swap_b32 v13, v15\n\t"
      "v_pk_add_f32 v[12:13], v[12:13], v[14:15]\n\t"
      "v_cvt_pk_bf16_f32 v12, v12, v13\n\t"
      "ds_write_b32 v6, v12 offset:6144\n\t"
      "s_waitcnt lgkmcnt(0)\n\t"
      "s_barrier\n\t"
      "s_mov_b32 s64, 0\n\t"
      "s_lshl_b32 s53, s48, 6\n\t"
      "s_lshl_b32 s54, s49, 3\n\t"
      "s_add_u32 s53, s53, s54\n\t"
      "s_add_u32 s53, s53, 266240\n\t"
      "s_sub_u32 s51, s43, 4\n\t"
      "s_cmp_ge_u32 s51, 10\n\t"
      "s_cselect_b32 s60, 1, 0\n\t"
      "s_cselect_b32 s54, 10, 0\n\t"
      "s_sub_u32 s61, s51, s54\n\t"
      "s_lshl_b32 s54, s42, 1\n\t"
      "s_add_u32 s60, s60, s54\n\t"
      "s_mul_i32 s54, s60, 2560\n\t"
      "s_lshl_b32 s55, s61, 8\n\t"
      "s_add_u32 s54, s54, s55\n\t"
      "s_add_u32 s54, s54, 268288\n\t"
      "s_barrier\n\t"
      "ds_read_b64 v[10:11], v5 offset:6400\n\t"
      "ds_read_b128 v[32:35], v7 offset:0\n\t"
      "ds_read_b128 v[36:39], v7 offset:512\n\t"
      "ds_read_b128 v[40:43], v7 offset:1024\n\t"
      "ds_read_b128 v[44:47], v7 offset:1536\n\t"
      "ds_read_b128 v[48:51], v7 offset:2048\n\t"
      "ds_read_b128 v[52:55], v7 offset:2560\n\t"
      "s_waitcnt lgkmcnt(6)\n\t"
      "v_max_f32_dpp v28, v10, v10 quad_perm:[1,0,3,2] row_mask:0xf bank_mask:0xf\n\t"
      "s_nop 1\n\t"
      "v_max_f32_dpp v28, v28, v28 quad_perm:[2,3,0,1] row_mask:0xf bank_mask:0xf\n\t"
      "s_nop 1\n\t"
      "v_max_f32_dpp v28, v28, v28 row_half_mirror row_mask:0xf bank_mask:0xf\n\t"
      "s_nop 1\n\t"
      "v_max_f32_dpp v28, v28, v28 row_ror:8 row_mask:0xf bank_mask:0xf\n\t"
      "v_sub_f32 v10, v10, v28\n\t"
      "v_mul_f32 v10, 0x3fb8aa3b, v10\n\t"
      "v_exp_f32 v10, v10\n\t"
      "v_and_or_b32 v28, v28, s47, v9\n\t"
      "s_waitcnt lgkmcnt(0)\n\t"
      "v_mul_f32 v29, v10, v11\n\t"
      "v_mul_f32_dpp v60, v10, v32 row_newbcast:0 row_mask:0xf bank_mask:0xf\n\t"
      "v_mul_f32_dpp v61, v10, v33 row_newbcast:0 row_mask:0xf bank_mask:0xf\n\t"
      "v_mul_f32_dpp v62, v10, v34 row_newbcast:0 row_mask:0xf bank_mask:0xf\n\t"
      "v_mul_f32_dpp v63, v10, v35 row_newbcast:0 row_mask:0xf bank_mask:0xf\n\t"
      "v_add_f32_dpp v29, v29, v29 quad_perm:[1,0,3,2] row_mask:0xf bank_mask:0xf\n\t"
      "v_fmac_f32_dpp v60, v10, v36 row_newbcast:1 row_mask:0xf bank_mask:0xf\n\t"
      "v_fmac_f32_dpp v61, v10, v37 row_newbcast:1 row_mask:0xf bank_mask:0xf\n\t"
      "v_fmac_f32_dpp v62, v10, v38 row_newbcast:1 row_mask:0xf bank_mask:0xf\n\t"
      "v_fmac_f32_dpp v63, v10, v39 row_newbcast:1 row_mask:0xf bank_mask:0xf\n\t"
      "v_add_f32_dpp v29, v29, v29 quad_perm:[2,3,0,1] row_mask:0xf bank_mask:0xf\n\t"
      "v_fmac_f32_dpp v60, v10, v40 row_newbcast:2 row_mask:0xf bank_mask:0xf\n\t"
      "v_fmac_f32_dpp v61, v10, v41 row_newbcast:2 row_mask:0xf bank_mask:0xf\n\t"
      "v_fmac_f32_dpp v62, v10, v42 row_newbcast:2 row_mask:0xf bank_mask:0xf\n\t"
      "v_fmac_f32_dpp v63, v10, v43 row_newbcast:2 row_mask:0xf bank_mask:0xf\n\t"
      "v_add_f32_dpp v29, v29, v29 row_half_mirror row_mask:0xf bank_mask:0xf\n\t"
      "v_fmac_f32_dpp v60, v10, v44 row_newbcast:3 row_mask:0xf bank_mask:0xf\n\t"
      "v_fmac_f32_dpp v61, v10, v45 row_newbcast:3 row_mask:0xf bank_mask:0xf\n\t"
      "v_fmac_f32_dpp v62, v10, v46 row_newbcast:3 row_mask:0xf bank_mask:0xf\n\t"
      "v_fmac_f32_dpp v63, v10, v47 row_newbcast:3 row_mask:0xf bank_mask:0xf\n\t"
      "v_add_f32_dpp v29, v29, v29 row_ror:8 row_mask:0xf bank_mask:0xf\n\t"
      "v_fmac_f32_dpp v60, v10, v48 row_newbcast:4 row_mask:0xf bank_mask:0xf\n\t"
      "v_fmac_f32_dpp v61, v10, v49 row_newbcast:4 row_mask:0xf bank_mask:0xf\n\t"
      "v_fmac_f32_dpp v62, v10, v50 row_newbcast:4 row_mask:0xf bank_mask:0xf\n\t"
      "v_fmac_f32_dpp v63, v10, v51 row_newbcast:4 row_mask:0xf bank_mask:0xf\n\t"
      "v_fmac_f32_dpp v60, v10, v52 row_newbcast:5 row_mask:0xf bank_mask:0xf\n\t"
      "v_fmac_f32_dpp v61, v10, v53 row_newbcast:5 row_mask:0xf bank_mask:0xf\n\t"
      "v_fmac_f32_dpp v62, v10, v54 row_newbcast:5 row_mask:0xf bank_mask:0xf\n\t"
      "v_fmac_f32_dpp v63, v10, v55 row_newbcast:5 row_mask:0xf bank_mask:0xf\n\t"
      "v_and_or_b32 v29, v29, s47, v9\n\t"
      "v_permlane32_swap_b32 v60, v62\n\t"
      "v_permlane32_swap_b32 v61, v63\n\t"
      "v_pk_add_f32 v[60:61], v[60:61], v[62:63]\n\t"
      "v_and_or_b32 v60, v60, s47, v9\n\t"
      "v_and_or_b32 v61, v61, s47, v9\n\t"
      "buffer_store_dwordx2 v[60:61], v8, s[12:15], s52 offen sc1\n\t"
      "s_mov_b64 exec, 1\n\t"
      "buffer_store_dwordx2 v[28:29], off, s[12:15], s53 sc1\n\t"
      "s_mov_b64 exec, -1\n\t"
      "s_cmp_lt_u32 s43, 4\n\t"
      "s_cbranch_scc1 .Lk_rd\n\t"
      "s_barrier\n\t"
      "s_barrier\n\t"
      "ds_read2st64_b32 v[10:11], v3 offset0:0 offset1:1\n\t"
      "ds_read2st64_b32 v[12:13], v3 offset0:2 offset1:3\n\t"
      "ds_read2st64_b32 v[14:15], v3 offset0:4 offset1:5\n\t"
      "ds_read2st64_b32 v[16:17], v3 offset0:6 offset1:7\n\t"
      "s_waitcnt lgkmcnt(2)\n\t"
      "v_pk_add_f32 v[10:11], v[10:11], v[12:13]\n\t"
      "s_waitcnt lgkmcnt(0)\n\t"
      "v_pk_add_f32 v[14:15], v[14:15], v[16:17]\n\t"
      "v_pk_add_f32 v[10:11], v[10:11], v[14:15]\n\t"
      "v_add_f32 v10, v10, v11\n\t"
      "v_and_or_b32 v10, v10, s47, v9\n\t"
      "buffer_store_dword v10, v3, s[12:15], s54 offen sc1\n\t"
      "s_endpgm\n\t"
      ".Lk_qbad:\n\t"
      "s_sleep 8\n\t"
      "s_add_u32 s64, s64, 1\n\t"
      "s_cmp_lt_u32 s64, 262144\n\t"
      "s_cbranch_scc1 .Lk_qpoll\n\t"
      "s_branch .Lk_qok\n\t"
      ".Lk_other:\n\t"
      "s_waitcnt lgkmcnt(0)\n\t"
      "s_load_dword s45, s[12:13], 0x0\n\t"
      "s_cmp_lg_u32 s43, 31\n\t"
      "s_cbranch_scc1 .Lk_nopf\n\t"
      "s_cmp_lg_u32 s44, 9\n\t"
      "s_cbranch_scc1 .Lk_nopf\n\t"
      "s_getpc_b64 s[50:51]\n\t"
      ".Lk_pc:\n\t"
      "s_sub_u32 s50, s50, .Lk_pc-.Lk_start\n\t"
      "s_subb_u32 s51, s51, 0\n\t"
      "v_lshlrev_b32 v2, 7, v1\n\t"
      "global_load_dword v120, v2, s[50:51]\n\t"
      "v_add_u32 v2, 0x2000, v2\n\t"
      "v_cmp_gt_u32 vcc, .Lk_codeend-.Lk_start, v2\n\t"
      "s_and_saveexec_b64 s[52:53], vcc\n\t"
      "s_cbranch_execz .Lk_pf2\n\t"
      "global_load_dword v121, v2, s[50:51]\n\t"
      ".Lk_pf2:\n\t"
      "s_mov_b64 exec, -1\n\t"
      ".Lk_nopf:\n\t"
      "s_waitcnt lgkmcnt(0)\n\t"
      "s_add_u32 s45, s45, 1\n\t"
      "s_and_b32 s46, s45, 0xff\n\t"
      "s_cmp_eq_u32 s43, 31\n\t"
      "s_cbranch_scc0 .Lk_ffq\n\t"
      "s_cmp_eq_u32 s44, 0\n\t"
      "s_cbranch_scc0 .Lk_end\n\t"
      ".Lk_rd:\n\t"
      "s_mul_i32 s51, s42, 5\n\t"
      "s_min_u32 s52, s43, 4\n\t"
      "s_add_u32 s51, s51, s52\n\t"
      "v_and_b32 v4, 3, v1\n\t"
      "v_lshrrev_b32 v5, 4, v1\n\t"
      "v_lshlrev_b32 v5, 1, v5\n\t"
      "v_lshl_or_b32 v2, v4, 3, v5\n\t"
      "s_lshl_b32 s52, s51, 5\n\t"
      "buffer_load_ushort v3, v2, s[16:19], s52 offen\n\t"
      "buffer_load_dword v6, off, s[36:39], s52\n\t"
      "v_lshrrev_b32 v5, 2, v1\n\t"
      "v_lshlrev_b32 v4, 4, v4\n\t"
      "s_movk_i32 s53, 2560\n\t"
      "v_min_u32 v8, 5, v5\n\t"
      "v_mad_u32_u24 v8, v8, s53, v4\n\t"
      "v_mad_u32_u24 v4, v5, s53, v4\n\t"
      "v_mov_b32 v10, s47\n\t"
      "s_lshl_b32 s54, s51, 6\n\t"
      "s_add_u32 s55, s54, 309248\n\t"
      "v_cmp_gt_u32 vcc, 24, v1\n\t"
      "s_add_u32 s54, s54, 268288\n\t"
      "s_add_u32 s56, s55, 40960\n\t"
      "v_cndmask_b32 v10, 0, v10, vcc\n\t"
      "s_add_u32 s57, s56, 40960\n\t"
      "s_add_u32 s58, s57, 40960\n\t"
      "s_mov_b32 s60, 0x000f000f\n\t"
      "s_mov_b32 s61, 0x000f000f\n\t"
      ".Lk_rd1:\n\t"
      "buffer_load_dwordx4 v[12:15], v4, s[12:15], s54 offen sc1\n\t"
      "s_waitcnt vmcnt(0)\n\t"
      "v_xor_b32 v30, s46, v12\n\t"
      "v_bitop3_b32 v30, v30, v13, s46 bitop3:0xf6\n\t"
      "v_bitop3_b32 v30, v30, v14, s46 bitop3:0xf6\n\t"
      "v_bitop3_b32 v30, v30, v15, s46 bitop3:0xf6\n\t"
      "v_cmp_ne_u32_sdwa vcc, v30, 0 src0_sel:BYTE_0 src1_sel:DWORD\n\t"
      "s_cbranch_vccz .Lk_rd1ok\n\t"
      "s_sleep 2\n\t"
      "s_add_u32 s64, s64, 1\n\t"
      "s_cmp_lt_u32 s64, 262144\n\t"
      "s_cbranch_scc1 .Lk_rd1\n\t"
      "s_branch .Lk_rd1ok\n\t"
      ".Lk_rd1ok:\n\t"
      "v_and_b32 v12, s47, v12\n\t"
      "v_and_b32 v13, s47, v13\n\t"
      "v_and_b32 v14, s47, v14\n\t"
      "v_and_b32 v15, s47, v15\n\t"
      "s_nop 0\n\t"
      "v_permlane32_swap_b32 v12, v14\n\t"
      "v_permlane32_swap_b32 v13, v15\n\t"
      "v_pk_add_f32 v[12:13], v[12:13], v[14:15]\n\t"
      "s_nop 1\n\t"
      "v_permlane16_swap_b32 v12, v13\n\t"
      "v_add_f32 v12, v12, v13\n\t"
      "s_nop 1\n\t"
      "v_add_f32_dpp v12, v12, v12 row_ror:4 row_mask:0xf bank_mask:0xf\n\t"
      "v_lshlrev_b32 v3, 16, v3\n\t"
      "s_mov_b32 s64, 0\n\t"
      "v_add_f32_dpp v12, v12, v12 row_ror:8 row_mask:0xf bank_mask:0xf\n\t"
      "v_cvt_pk_bf16_f32 v12, v12, v12\n\t"
      "v_lshlrev_b32 v12, 16, v12\n\t"
      "v_add_f32 v3, v3, v12\n\t"
      "v_cvt_pk_bf16_f32 v3, v3, v3\n\t"
      "v_lshlrev_b32 v3, 16, v3\n\t"
      ".Lk_rd2:\n\t"
      "buffer_load_dwordx4 v[12:15], v4, s[12:15], s55 offen sc1\n\t"
      "buffer_load_dwordx4 v[16:19], v4, s[12:15], s56 offen sc1\n\t"
      "buffer_load_dwordx4 v[20:23], v4, s[12:15], s57 offen sc1\n\t"
      "buffer_load_dwordx4 v[24:27], v8, s[12:15], s58 offen sc1\n\t"
      "s_waitcnt vmcnt(3)\n\t"
      "v_xor_b32 v30, s46, v12\n\t"
      "v_bitop3_b32 v30, v30, v13, s46 bitop3:0xf6\n\t"
      "v_bitop3_b32 v30, v30, v14, s46 bitop3:0xf6\n\t"
      "v_bitop3_b32 v30, v30, v15, s46 bitop3:0xf6\n\t"
      "v_and_b32 v12, s47, v12\n\t"
      "v_and_b32 v13, s47, v13\n\t"
      "v_and_b32 v14, s47, v14\n\t"
      "v_and_b32 v15, s47, v15\n\t"
      "s_waitcnt vmcnt(2)\n\t"
      "v_bitop3_b32 v30, v30, v16, s46 bitop3:0xf6\n\t"
      "v_bitop3_b32 v30, v30, v17, s46 bitop3:0xf6\n\t"
      "v_bitop3_b32 v30, v30, v18, s46 bitop3:0xf6\n\t"
      "v_bitop3_b32 v30, v30, v19, s46 bitop3:0xf6\n\t"
      "v_and_b32 v16, s47, v16\n\t"
      "v_and_b32 v17, s47, v17\n\t"
      "v_and_b32 v18, s47, v18\n\t"
      "v_and_b32 v19, s47, v19\n\t"
      "v_pk_add_f32 v[12:13], v[12:13], v[16:17]\n\t"
      "v_pk_add_f32 v[14:15], v[14:15], v[18:19]\n\t"
      "s_waitcnt vmcnt(1)\n\t"
      "v_bitop3_b32 v30, v30, v20, s46 bitop3:0xf6\n\t"
      "v_bitop3_b32 v30, v30, v21, s46 bitop3:0xf6\n\t"
      "v_bitop3_b32 v30, v30, v22, s46 bitop3:0xf6\n\t"
      "v_bitop3_b32 v30, v30, v23, s46 bitop3:0xf6\n\t"
      "v_and_b32 v20, s47, v20\n\t"
      "v_and_b32 v21, s47, v21\n\t"
      "v_and_b32 v22, s47, v22\n\t"
      "v_and_b32 v23, s47, v23\n\t"
      "v_pk_add_f32 v[12:13], v[12:13], v[20:21]\n\t"
      "v_pk_add_f32 v[14:15], v[14:15], v[22:23]\n\t"
      "s_waitcnt vmcnt(0)\n\t"
      "v_bitop3_b32 v30, v30, v24, s46 bitop3:0xf6\n\t"
      "v_bitop3_b32 v30, v30, v25, s46 bitop3:0xf6\n\t"
      "v_bitop3_b32 v30, v30, v26, s46 bitop3:0xf6\n\t"
      "v_bitop3_b32 v30, v30, v27, s46 bitop3:0xf6\n\t"
      "v_and_b32 v24, v10, v24\n\t"
      "v_and_b32 v25, v10, v25\n\t"
      "v_and_b32 v26, v10, v26\n\t"
      "v_and_b32 v27, v10, v27\n\t"
      "v_pk_add_f32 v[12:13], v[12:13], v[24:25]\n\t"
      "v_pk_add_f32 v[14:15], v[14:15], v[26:27]\n\t"
      "v_cmp_ne_u32_sdwa vcc, v30, 0 src0_sel:BYTE_0 src1_sel:DWORD\n\t"
      "s_cbranch_vccnz .Lk_rd2bad\n\t"
      ".Lk_rd2ok:\n\t"
      "v_permlane32_swap_b32 v12, v14\n\t"
      "v_permlane32_swap_b32 v13, v15\n\t"
      "v_pk_add_f32 v[12:13], v[12:13], v[14:15]\n\t"
      "s_nop 1\n\t"
      "v_permlane16_swap_b32 v12, v13\n\t"
      "v_add_f32 v12, v12, v13\n\t"
      "s_nop 1\n\t"
      "v_add_f32_dpp v12, v12, v12 row_ror:4 row_mask:0xf bank_mask:0xf\n\t"
      "s_nop 1\n\t"
      "v_add_f32_dpp v12, v12, v12 row_ror:8 row_mask:0xf bank_mask:0xf\n\t"
      "v_cvt_pk_bf16_f32 v12, v12, v12\n\t"
      "v_lshlrev_b32 v12, 16, v12\n\t"
      "v_add_f32 v3, v3, v12\n\t"
      "v_cvt_pk_bf16_f32 v3, v3, v3\n\t"
      "s_mov_b64 exec, s[60:61]\n\t"
      "buffer_store_short v3, v2, s[36:39], s52 offen sc1\n\t"
      "s_cmp_lg_u32 s51, 0\n\t"
      "s_cbranch_scc1 .Lk_rdend\n\t"
      "s_mov_b64 exec, 1\n\t"
      "v_mov_b32 v4, s45\n\t"
      "buffer_store_dword v4, off, s[12:15], 0 sc1\n\t"
      ".Lk_rdend:\n\t"
      "s_mov_b64 exec, -1\n\t"
      "s_endpgm\n\t"
      ".Lk_rd2bad:\n\t"
      "s_sleep 1\n\t"
      "s_add_u32 s64, s64, 1\n\t"
      "s_cmp_lt_u32 s64, 262144\n\t"
      "s_cbranch_scc1 .Lk_rd2\n\t"
      "s_branch .Lk_rd2ok\n\t"
      ".Lk_ffq:\n\t"
      "s_mul_i32 s49, s42, 7\n\t"
      "s_add_u32 s49, s49, s43\n\t"
      "s_sub_u32 s49, s49, 24\n\t"
      "s_cmp_ge_u32 s49, 54\n\t"
      "s_cbranch_scc1 .Lk_end3\n\t"
      "s_cmp_ge_u32 s44, 10\n\t"
      "s_cbranch_scc1 .Lk_end3\n\t"
      "v_and_b32 v2, 15, v1\n\t"
      "v_lshrrev_b32 v107, 4, v1\n\t"
      "v_lshlrev_b32 v100, 1, v107\n\t"
      "v_lshl_or_b32 v100, v2, 3, v100\n\t"
      "s_lshl_b32 s51, s44, 7\n\t"
      "v_lshlrev_b32 v2, 4, v2\n\t"
      "s_movk_i32 s53, 2560\n\t"
      "v_mad_u32_u24 v102, v107, s53, v2\n\t"
      "s_lshl_b32 s54, s44, 8\n\t"
      "s_add_u32 s54, s54, 268288\n\t"
      "s_add_u32 s55, s54, 10240\n\t"
      "s_add_u32 s56, s55, 10240\n\t"
      "s_add_u32 s57, s56, 10240\n\t"
      "s_lshl_b32 s50, s44, 3\n\t"
      "v_mov_b32 v105, s50\n\t"
      "v_mov_b32 v106, s46\n\t"
      "buffer_load_ushort v101, v100, s[16:19], s51 offen\n\t"
      "v_add_u32 v100, s51, v100\n\t"
      "s_cmp_ge_u32 s44, 8\n\t"
      "s_cbranch_scc1 .Lk_ff1\n\t"
      "v_lshrrev_b32 v2, 2, v1\n\t"
      "v_and_b32 v3, 3, v1\n\t"
      "v_mul_u32_u24 v2, 3456, v2\n\t"
      "v_lshl_or_b32 v2, v3, 4, v2\n\t"
      "v_lshlrev_b32 v3, 4, v1\n\t"
      "s_mul_i32 s50, s44, 276480\n\t"
      "s_lshl_b32 s51, s49, 6\n\t"
      "s_add_u32 s58, s50, s51\n\t"
      "s_add_u32 s59, s58, 55296\n\t"
      "s_add_u32 s60, s59, 55296\n\t"
      "s_add_u32 s61, s60, 55296\n\t"
      "s_add_u32 s62, s61, 55296\n\t"
      "s_mul_i32 s50, s49, 40960\n\t"
      "s_mul_i32 s51, s44, 5120\n\t"
      "s_add_u32 s63, s50, s51\n\t"
      "s_add_u32 s65, s63, 4096\n\t"
      "v_add_u32 v45, s51, v3\n\t"
      ".Lk_fft:\n\t"
      "s_memrealtime s[66:67]\n\t"
      "s_waitcnt lgkmcnt(0)\n\t"
      "s_sub_u32 s66, s66, s40\n\t"
      "s_cmp_ge_u32 s66, 350\n\t"
      "s_cbranch_scc1 .Lk_fft_done\n\t"
      "s_sleep 2\n\t"
      "s_branch .Lk_fft\n\t"
      ".Lk_fft_done:\n\t"
      "buffer_load_dwordx4 v[4:7], v2, s[28:31], s58 offen nt\n\t"
      "buffer_load_dwordx4 v[8:11], v2, s[28:31], s59 offen nt\n\t"
      "buffer_load_dwordx4 v[12:15], v2, s[28:31], s60 offen nt\n\t"
      "buffer_load_dwordx4 v[16:19], v2, s[28:31], s61 offen nt\n\t"
      "buffer_load_dwordx4 v[20:23], v2, s[28:31], s62 offen nt\n\t"
      "buffer_load_dwordx4 v[24:27], v3, s[32:35], s63 offen offset:0 nt\n\t"
      "buffer_load_dwordx4 v[28:31], v3, s[32:35], s63 offen offset:1024 nt\n\t"
      "buffer_load_dwordx4 v[32:35], v3, s[32:35], s63 offen offset:2048 nt\n\t"
      "buffer_load_dwordx4 v[36:39], v3, s[32:35], s63 offen offset:3072 nt\n\t"
      "buffer_load_dwordx4 v[40:43], v3, s[32:35], s65 offen offset:0 nt\n\t"
      "s_mul_i32 s50, s44, 5248\n\t"
      "s_lshr_b32 s51, s44, 1\n\t"
      "s_lshl_b32 s51, s51, 7\n\t"
      "s_add_u32 s50, s50, s51\n\t"
      "v_add_u32 v46, 0, v1\n\t"
      "v_mul_u32_u24 v47, 205, v46\n\t"
      "v_lshrrev_b32 v47, 14, v47\n\t"
      "v_lshlrev_b32 v47, 5, v47\n\t"
      "v_lshl_add_u32 v46, v46, 4, v47\n\t"
      "v_add_u32 v48, s50, v46\n\t"
      "v_add_u32 v46, 64, v1\n\t"
      "v_mul_u32_u24 v47, 205, v46\n\t"
      "v_lshrrev_b32 v47, 14, v47\n\t"
      "v_lshlrev_b32 v47, 5, v47\n\t"
      "v_lshl_add_u32 v46, v46, 4, v47\n\t"
      "v_add_u32 v49, s50, v46\n\t"
      "v_add_u32 v46, 128, v1\n\t"
      "v_mul_u32_u24 v47, 205, v46\n\t"
      "v_lshrrev_b32 v47, 14, v47\n\t"
      "v_lshlrev_b32 v47, 5, v47\n\t"
      "v_lshl_add_u32 v46, v46, 4, v47\n\t"
      "v_add_u32 v50, s50, v46\n\t"
      "v_add_u32 v46, 192, v1\n\t"
      "v_mul_u32_u24 v47, 205, v46\n\t"
      "v_lshrrev_b32 v47, 14, v47\n\t"
      "v_lshlrev_b32 v47, 5, v47\n\t"
      "v_lshl_add_u32 v46, v46, 4, v47\n\t"
      "v_add_u32 v51, s50, v46\n\t"
      "v_add_u32 v46, 256, v1\n\t"
      "v_mul_u32_u24 v47, 205, v46\n\t"
      "v_lshrrev_b32 v47, 14, v47\n\t"
      "v_lshlrev_b32 v47, 5, v47\n\t"
      "v_lshl_add_u32 v46, v46, 4, v47\n\t"
      "v_add_u32 v52, s50, v46\n\t"
      "s_waitcnt vmcnt(9)\n\t"
      "ds_write_b128 v45, v[4:7] offset:10240\n\t"
      "s_waitcnt vmcnt(8)\n\t"
      "ds_write_b128 v45, v[8:11] offset:11264\n\t"
      "s_waitcnt vmcnt(7)\n\t"
      "ds_write_b128 v45, v[12:15] offset:12288\n\t"
      "s_waitcnt vmcnt(6)\n\t"
      "ds_write_b128 v45, v[16:19] offset:13312\n\t"
      "s_waitcnt vmcnt(5)\n\t"
      "ds_write_b128 v45, v[20:23] offset:14336\n\t"
      "s_waitcnt vmcnt(4)\n\t"
      "ds_write_b128 v48, v[24:27] offset:51200\n\t"
      "s_waitcnt vmcnt(3)\n\t"
      "ds_write_b128 v49, v[28:31] offset:51200\n\t"
      "s_waitcnt vmcnt(2)\n\t"
      "ds_write_b128 v50, v[32:35] offset:51200\n\t"
      "s_waitcnt vmcnt(1)\n\t"
      "ds_write_b128 v51, v[36:39] offset:51200\n\t"
      "s_waitcnt vmcnt(0)\n\t"
      "ds_write_b128 v52, v[40:43] offset:51200\n\t"
      ".Lk_ff1:\n\t"
      "s_waitcnt lgkmcnt(0)\n\t"
      "s_barrier\n\t"
      "v_bfe_u32 v3, v1, 2, 2\n\t"
      "v_lshl_or_b32 v2, v107, 3, v3\n\t"
      "v_and_b32 v3, 3, v1\n\t"
      "v_lshlrev_b32 v3, 3, v3\n\t"
      "s_cmp_ge_u32 s44, 8\n\t"
      "s_cbranch_scc1 .Lk_ff3\n\t"
      "v_mul_u32_u24 v44, 1312, v2\n\t"
      "v_lshl_add_u32 v44, v107, 7, v44\n\t"
      "s_mul_i32 s50, s44, 160\n\t"
      "v_add3_u32 v44, v44, s50, v3\n\t"
      "ds_read_b64_tr_b16 v[80:81], v44 offset:51200\n\t"
      "ds_read_b64_tr_b16 v[82:83], v44 offset:56448\n\t"
      "ds_read_b64_tr_b16 v[84:85], v44 offset:51232\n\t"
      "ds_read_b64_tr_b16 v[86:87], v44 offset:56480\n\t"
      "ds_read_b64_tr_b16 v[88:89], v44 offset:51264\n\t"
      "ds_read_b64_tr_b16 v[90:91], v44 offset:56512\n\t"
      "ds_read_b64_tr_b16 v[92:93], v44 offset:51296\n\t"
      "ds_read_b64_tr_b16 v[94:95], v44 offset:56544\n\t"
      "ds_read_b64_tr_b16 v[96:97], v44 offset:51328\n\t"
      "ds_read_b64_tr_b16 v[98:99], v44 offset:56576\n\t"
      "v_lshlrev_b32 v108, 5, v107\n\t"
      "v_lshlrev_b32 v109, 2, v1\n\t"
      "s_mul_i32 s58, s49, 2560\n\t"
      "s_mul_i32 s50, s44, 320\n\t"
      "s_add_u32 s58, s58, s50\n\t"
      "s_add_u32 s58, s58, 309248\n\t"
      "v_cmp_eq_u32 s[60:61], 1, v107\n\t"
      "v_cmp_eq_u32 s[62:63], 2, v107\n\t"
      "v_cmp_eq_u32 s[66:67], 3, v107\n\t"
      "s_cmp_ge_u32 s44, 4\n\t"
      "s_cbranch_scc1 .Lk_ff3\n\t"
      "v_lshl_or_b32 v44, v2, 6, v3\n\t"
      "s_lshr_b32 s50, s44, 1\n\t"
      "s_mul_i32 s51, s50, 20480\n\t"
      "s_and_b32 s52, s44, 1\n\t"
      "s_lshl_b32 s53, s52, 5\n\t"
      "s_add_u32 s51, s51, s53\n\t"
      "v_add_u32 v44, s51, v44\n\t"
      "ds_read_b64_tr_b16 v[4:5], v44 offset:10240\n\t"
      "ds_read_b64_tr_b16 v[6:7], v44 offset:10496\n\t"
      "ds_read_b64_tr_b16 v[8:9], v44 offset:12288\n\t"
      "ds_read_b64_tr_b16 v[10:11], v44 offset:12544\n\t"
      "ds_read_b64_tr_b16 v[12:13], v44 offset:14336\n\t"
      "ds_read_b64_tr_b16 v[14:15], v44 offset:14592\n\t"
      "ds_read_b64_tr_b16 v[16:17], v44 offset:16384\n\t"
      "ds_read_b64_tr_b16 v[18:19], v44 offset:16640\n\t"
      "s_waitcnt lgkmcnt(0)\n\t"
      "ds_read_b64_tr_b16 v[20:21], v44 offset:18432\n\t"
      "ds_read_b64_tr_b16 v[22:23], v44 offset:18688\n\t"
      "ds_read_b64_tr_b16 v[24:25], v44 offset:20480\n\t"
      "ds_read_b64_tr_b16 v[26:27], v44 offset:20736\n\t"
      "ds_read_b64_tr_b16 v[28:29], v44 offset:22528\n\t"
      "ds_read_b64_tr_b16 v[30:31], v44 offset:22784\n\t"
      "ds_read_b64_tr_b16 v[32:33], v44 offset:24576\n\t"
      "ds_read_b64_tr_b16 v[34:35], v44 offset:24832\n\t"
      "s_waitcnt lgkmcnt(0)\n\t"
      "ds_read_b64_tr_b16 v[36:37], v44 offset:26624\n\t"
      "ds_read_b64_tr_b16 v[38:39], v44 offset:26880\n\t"
      "ds_read_b64_tr_b16 v[40:41], v44 offset:28672\n\t"
      "ds_read_b64_tr_b16 v[42:43], v44 offset:28928\n\t"
      "v_mov_b32 v48, 0x3f803f80\n\t"
      "v_mov_b32 v49, v48\n\t"
      "v_mov_b32 v50, v48\n\t"
      "v_mov_b32 v51, v48\n\t"
      "s_mul_i32 s51, s50, 640\n\t"
      "v_lshl_add_u32 v110, v107, 4, s51\n\t"
      "s_lshl_b32 s50, s50, 7\n\t"
      "s_lshl_b32 s52, s52, 6\n\t"
      "s_add_u32 s50, s50, s52\n\t"
      "v_add_u32 v111, s50, v109\n\t"
      "v_mov_b32 v104, 0\n\t"
      "s_waitcnt lgkmcnt(0)\n\t"
      "v_mfma_f32_16x16x32_bf16 v[116:119], v[48:51], v[4:7], 0\n\t"
      "v_mfma_f32_16x16x32_bf16 v[116:119], v[48:51], v[8:11], v[116:119]\n\t"
      "v_mfma_f32_16x16x32_bf16 v[116:119], v[48:51], v[12:15], v[116:119]\n\t"
      "v_mfma_f32_16x16x32_bf16 v[116:119], v[48:51], v[16:19], v[116:119]\n\t"
      "v_mfma_f32_16x16x32_bf16 v[116:119], v[48:51], v[20:23], v[116:119]\n\t"
      "v_mfma_f32_16x16x32_bf16 v[116:119], v[48:51], v[24:27], v[116:119]\n\t"
      "v_mfma_f32_16x16x32_bf16 v[116:119], v[48:51], v[28:31], v[116:119]\n\t"
      "v_mfma_f32_16x16x32_bf16 v[116:119], v[48:51], v[32:35], v[116:119]\n\t"
      "v_mfma_f32_16x16x32_bf16 v[116:119], v[48:51], v[36:39], v[116:119]\n\t"
      "v_mfma_f32_16x16x32_bf16 v[116:119], v[48:51], v[40:43], v[116:119]\n\t"
      ".Lk_ff3:\n\t"
      "s_waitcnt vmcnt(0) lgkmcnt(0)\n\t"
      "v_lshlrev_b32 v101, 16, v101\n\t"
      ".Lk_ffp:\n\t"
      "buffer_load_dwordx4 v[44:47], v102, s[12:15], s54 offen sc1\n\t"
      "buffer_load_dwordx4 v[48:51], v102, s[12:15], s55 offen sc1\n\t"
      "buffer_load_dwordx4 v[52:55], v102, s[12:15], s56 offen sc1\n\t"
      "buffer_load_dwordx4 v[56:59], v102, s[12:15], s57 offen sc1\n\t"
      "s_waitcnt vmcnt(3)\n\t"
      "v_xor_b32 v60, s46, v44\n\t"
      "v_bitop3_b32 v60, v60, v45, s46 bitop3:0xf6\n\t"
      "v_bitop3_b32 v60, v60, v46, s46 bitop3:0xf6\n\t"
      "v_bitop3_b32 v60, v60, v47, s46 bitop3:0xf6\n\t"
      "v_and_b32 v44, s47, v44\n\t"
      "v_and_b32 v45, s47, v45\n\t"
      "v_and_b32 v46, s47, v46\n\t"
      "v_and_b32 v47, s47, v47\n\t"
      "s_waitcnt vmcnt(2)\n\t"
      "v_bitop3_b32 v60, v60, v48, s46 bitop3:0xf6\n\t"
      "v_bitop3_b32 v60, v60, v49, s46 bitop3:0xf6\n\t"
      "v_bitop3_b32 v60, v60, v50, s46 bitop3:0xf6\n\t"
      "v_bitop3_b32 v60, v60, v51, s46 bitop3:0xf6\n\t"
      "v_and_b32 v48, s47, v48\n\t"
      "v_and_b32 v49, s47, v49\n\t"
      "v_and_b32 v50, s47, v50\n\t"
      "v_and_b32 v51, s47, v51\n\t"
      "v_pk_add_f32 v[44:45], v[44:45], v[48:49]\n\t"
      "v_pk_add_f32 v[46:47], v[46:47], v[50:51]\n\t"
      "s_waitcnt vmcnt(1)\n\t"
      "v_bitop3_b32 v60, v60, v52, s46 bitop3:0xf6\n\t"
      "v_bitop3_b32 v60, v60, v53, s46 bitop3:0xf6\n\t"
      "v_bitop3_b32 v60, v60, v54, s46 bitop3:0xf6\n\t"
      "v_bitop3_b32 v60, v60, v55, s46 bitop3:0xf6\n\t"
      "v_and_b32 v52, s47, v52\n\t"
      "v_and_b32 v53, s47, v53\n\t"
      "v_and_b32 v54, s47, v54\n\t"
      "v_and_b32 v55, s47, v55\n\t"
      "v_pk_add_f32 v[44:45], v[44:45], v[52:53]\n\t"
      "v_pk_add_f32 v[46:47], v[46:47], v[54:55]\n\t"
      "s_waitcnt vmcnt(0)\n\t"
      "v_bitop3_b32 v60, v60, v56, s46 bitop3:0xf6\n\t"
      "v_bitop3_b32 v60, v60, v57, s46 bitop3:0xf6\n\t"
      "v_bitop3_b32 v60, v60, v58, s46 bitop3:0xf6\n\t"
      "v_bitop3_b32 v60, v60, v59, s46 bitop3:0xf6\n\t"
      "v_and_b32 v56, s47, v56\n\t"
      "v_and_b32 v57, s47, v57\n\t"
      "v_and_b32 v58, s47, v58\n\t"
      "v_and_b32 v59, s47, v59\n\t"
      "v_pk_add_f32 v[44:45], v[44:45], v[56:57]\n\t"
      "v_pk_add_f32 v[46:47], v[46:47], v[58:59]\n\t"
      "v_cmp_ne_u32_sdwa vcc, v60, 0 src0_sel:BYTE_0 src1_sel:DWORD\n\t"
      "s_cbranch_vccnz .Lk_ffbad\n\t"
      ".Lk_ffok:\n\t"
      "v_permlane32_swap_b32 v44, v46\n\t"
      "v_permlane32_swap_b32 v45, v47\n\t"
      "v_pk_add_f32 v[44:45], v[44:45], v[46:47]\n\t"
      "s_nop 1\n\t"
      "v_permlane16_swap_b32 v44, v45\n\t"
      "v_add_f32 v44, v44, v45\n\t"
      "v_cvt_pk_bf16_f32 v44, v44, v44\n\t"
      "v_lshlrev_b32 v44, 16, v44\n\t"
      "v_add_f32 v44, v44, v101\n\t"
      "v_cvt_pk_bf16_f32 v44, v44, v44\n\t"
      "ds_write_b16 v100, v44 offset:6544\n\t"
      "v_lshlrev_b32 v46, 16, v44\n\t"
      "v_mul_f32 v47, v46, v46\n\t"
      "s_nop 0\n\t"
      "v_add_f32_dpp v46, v46, v46 quad_perm:[1,0,3,2] row_mask:0xf bank_mask:0xf\n\t"
      "v_add_f32_dpp v47, v47, v47 quad_perm:[1,0,3,2] row_mask:0xf bank_mask:0xf\n\t"
      "s_nop 0\n\t"
      "v_add_f32_dpp v46, v46, v46 quad_perm:[2,3,0,1] row_mask:0xf bank_mask:0xf\n\t"
      "v_add_f32_dpp v47, v47, v47 quad_perm:[2,3,0,1] row_mask:0xf bank_mask:0xf\n\t"
      "s_nop 0\n\t"
      "v_add_f32_dpp v46, v46, v46 row_half_mirror row_mask:0xf bank_mask:0xf\n\t"
      "v_add_f32_dpp v47, v47, v47 row_half_mirror row_mask:0xf bank_mask:0xf\n\t"
      "s_nop 0\n\t"
      "v_add_f32_dpp v46, v46, v46 row_ror:8 row_mask:0xf bank_mask:0xf\n\t"
      "v_add_f32_dpp v47, v47, v47 row_ror:8 row_mask:0xf bank_mask:0xf\n\t"
      "s_nop 1\n\t"
      "v_permlane16_swap_b32 v46, v47\n\t"
      "v_add_f32 v46, v46, v47\n\t"
      "v_mov_b32 v48, v46\n\t"
      "s_nop 1\n\t"
      "v_permlane32_swap_b32 v46, v48\n\t"
      "v_add_f32 v46, v46, v48\n\t"
      "v_mov_b32 v47, v46\n\t"
      "s_nop 1\n\t"
      "v_permlane16_swap_b32 v46, v47\n\t"
      "s_mov_b64 exec, 1\n\t"
      "ds_write_b64 v105, v[46:47] offset:7824\n\t"
      "s_mov_b64 exec, -1\n\t"
      "s_waitcnt lgkmcnt(0)\n\t"
      "s_barrier\n\t"
      "s_cmp_ge_u32 s44, 8\n\t"
      "s_cbranch_scc1 .Lk_end3\n\t"
      "s_cmp_ge_u32 s44, 4\n\t"
      "s_cbranch_scc1 .Lk_ff4\n\t"
      "ds_read_b128 v[44:47], v110 offset:6544\n\t"
      "ds_read_b128 v[48:51], v110 offset:6608\n\t"
      "ds_read_b128 v[124:127], v104 offset:7824\n\t"
      "ds_read_b128 v[128:131], v104 offset:7840\n\t"
      "ds_read_b128 v[132:135], v104 offset:7856\n\t"
      "ds_read_b128 v[136:139], v104 offset:7872\n\t"
      "ds_read_b128 v[140:143], v104 offset:7888\n\t"
      "ds_read_b128 v[52:55], v110 offset:6672\n\t"
      "ds_read_b128 v[56:59], v110 offset:6736\n\t"
      "ds_read_b128 v[60:63], v110 offset:6800\n\t"
      "ds_read_b128 v[64:67], v110 offset:6864\n\t"
      "ds_read_b128 v[68:71], v110 offset:6928\n\t"
      "ds_read_b128 v[72:75], v110 offset:6992\n\t"
      "ds_read_b128 v[76:79], v110 offset:7056\n\t"
      "ds_read_b128 v[120:123], v110 offset:7120\n\t"
      "s_waitcnt lgkmcnt(14)\n\t"
      "v_mfma_f32_16x16x32_bf16 v[112:115], v[44:47], v[4:7], 0\n\t"
      "s_waitcnt lgkmcnt(13)\n\t"
      "v_mfma_f32_16x16x32_bf16 v[112:115], v[48:51], v[8:11], v[112:115]\n\t"
      "s_waitcnt lgkmcnt(7)\n\t"
      "v_mfma_f32_16x16x32_bf16 v[112:115], v[52:55], v[12:15], v[112:115]\n\t"
      "v_pk_add_f32 v[124:125], v[124:125], v[126:127]\n\t"
      "v_pk_add_f32 v[128:129], v[128:129], v[130:131]\n\t"
      "v_pk_add_f32 v[132:133], v[132:133], v[134:135]\n\t"
      "s_waitcnt lgkmcnt(6)\n\t"
      "v_mfma_f32_16x16x32_bf16 v[112:115], v[56:59], v[16:19], v[112:115]\n\t"
      "v_pk_add_f32 v[136:137], v[136:137], v[138:139]\n\t"
      "v_pk_add_f32 v[140:141], v[140:141], v[142:143]\n\t"
      "v_pk_add_f32 v[124:125], v[124:125], v[128:129]\n\t"
      "s_waitcnt lgkmcnt(5)\n\t"
      "v_mfma_f32_16x16x32_bf16 v[112:115], v[60:63], v[20:23], v[112:115]\n\t"
      "v_pk_add_f32 v[132:133], v[132:133], v[136:137]\n\t"
      "v_pk_add_f32 v[124:125], v[124:125], v[140:141]\n\t"
      "v_pk_add_f32 v[124:125], v[124:125], v[132:133]\n\t"
      "s_waitcnt lgkmcnt(4)\n\t"
      "v_mfma_f32_16x16x32_bf16 v[112:115], v[64:67], v[24:27], v[112:115]\n\t"
      "v_mul_f32 v124, 0x3acccccd, v124\n\t"
      "v_mul_f32 v125, 0x3acccccd, v125\n\t"
      "v_fma_f32 v125, -v124, v124, v125\n\t"
      "s_waitcnt lgkmcnt(3)\n\t"
      "v_mfma_f32_16x16x32_bf16 v[112:115], v[68:71], v[28:31], v[112:115]\n\t"
      "v_cvt_pk_bf16_f32 v124, v124, v124\n\t"
      "v_max_f32 v125, 0, v125\n\t"
      "v_lshlrev_b32 v124, 16, v124\n\t"
      "s_waitcnt lgkmcnt(2)\n\t"
      "v_mfma_f32_16x16x32_bf16 v[112:115], v[72:75], v[32:35], v[112:115]\n\t"
      "v_cvt_pk_bf16_f32 v125, v125, v125\n\t"
      "v_lshlrev_b32 v125, 16, v125\n\t"
      "v_add_f32 v125, 0x3727c5ac, v125\n\t"
      "s_waitcnt lgkmcnt(1)\n\t"
      "v_mfma_f32_16x16x32_bf16 v[112:115], v[76:79], v[36:39], v[112:115]\n\t"
      "v_cvt_pk_bf16_f32 v125, v125, v125\n\t"
      "v_lshlrev_b32 v125, 16, v125\n\t"
      "v_sqrt_f32 v125, v125\n\t"
      "s_waitcnt lgkmcnt(0)\n\t"
      "v_mfma_f32_16x16x32_bf16 v[112:115], v[120:123], v[40:43], v[112:115]\n\t"
      "v_cvt_pk_bf16_f32 v125, v125, v125\n\t"
      "v_lshlrev_b32 v125, 16, v125\n\t"
      "v_rcp_f32 v125, v125\n\t"
      "s_mov_b64 exec, 0xffff\n\t"
      "s_nop 3\n\t"
      "v_fma_f32 v112, -v124, v116, v112\n\t"
      "v_mul_f32 v112, v112, v125\n\t"
      "ds_write_b32 v111, v112 offset:7904\n\t"
      "s_mov_b64 exec, -1\n\t"
      ".Lk_ff4:\n\t"
      "s_waitcnt lgkmcnt(0)\n\t"
      "s_barrier\n\t"
      "ds_read_b128 v[4:7], v108 offset:7904\n\t"
      "ds_read_b128 v[8:11], v108 offset:8032\n\t"
      "ds_read_b128 v[12:15], v108 offset:7920\n\t"
      "ds_read_b128 v[16:19], v108 offset:8048\n\t"
      "s_waitcnt lgkmcnt(2)\n\t"
      "v_pk_add_f32 v[4:5], v[4:5], v[8:9]\n\t"
      "v_pk_add_f32 v[6:7], v[6:7], v[10:11]\n\t"
      "s_waitcnt lgkmcnt(0)\n\t"
      "v_pk_add_f32 v[12:13], v[12:13], v[16:17]\n\t"
      "v_pk_add_f32 v[14:15], v[14:15], v[18:19]\n\t"
      "v_cvt_pk_bf16_f32 v4, v4, v5\n\t"
      "v_cvt_pk_bf16_f32 v5, v6, v7\n\t"
      "v_cvt_pk_bf16_f32 v6, v12, v13\n\t"
      "v_cvt_pk_bf16_f32 v7, v14, v15\n\t"
      "v_pk_max_i16 v4, v4, 0\n\t"
      "v_pk_max_i16 v5, v5, 0\n\t"
      "v_pk_max_i16 v6, v6, 0\n\t"
      "v_pk_max_i16 v7, v7, 0\n\t"
      "s_nop 1\n\t"
      "v_mfma_f32_16x16x32_bf16 v[12:15], v[4:7], v[80:83], 0\n\t"
      "v_mfma_f32_16x16x32_bf16 v[16:19], v[4:7], v[84:87], 0\n\t"
      "v_mfma_f32_16x16x32_bf16 v[20:23], v[4:7], v[88:91], 0\n\t"
      "v_mfma_f32_16x16x32_bf16 v[24:27], v[4:7], v[92:95], 0\n\t"
      "v_mfma_f32_16x16x32_bf16 v[28:31], v[4:7], v[96:99], 0\n\t"
      "s_nop 4\n\t"
      "v_cndmask_b32_e64 v12, v12, v16, s[60:61]\n\t"
      "v_cndmask_b32_e64 v12, v12, v20, s[62:63]\n\t"
      "v_cndmask_b32_e64 v12, v12, v24, s[66:67]\n\t"
      "v_and_or_b32 v12, v12, s47, v106\n\t"
      "buffer_store_dword v12, v109, s[12:15], s58 offen sc1\n\t"
      "v_and_or_b32 v28, v28, s47, v106\n\t"
      "s_mov_b64 exec, 0xffff\n\t"
      "buffer_store_dword v28, v109, s[12:15], s58 offen offset:256 sc1\n\t"
      "s_mov_b64 exec, -1\n\t"
      ".Lk_end3:\n\t"
      "s_endpgm\n\t"
      ".Lk_ffbad:\n\t"
      "s_sleep 1\n\t"
      "s_add_u32 s64, s64, 1\n\t"
      "s_cmp_lt_u32 s64, 262144\n\t"
      "s_cbranch_scc1 .Lk_ffp\n\t"
      "s_branch .Lk_ffok\n\t"
      ".Lk_codeend:\n\t"
      :
      : "s"(__builtin_amdgcn_kernarg_segment_ptr()), "s"(__builtin_amdgcn_workgroup_id_x()), "v"(__builtin_amdgcn_workitem_id_x()),
        "s"((unsigned)(uintptr_t)(__attribute__((address_space(3))) char*)lds)
      : "v1", "v2", "v3", "v4", "v5", "v6", "v7", "v8", "v9", "v10", "v11", "v12", "v13", "v14", "v15", "v16", "v17", "v18", "v19", "v20", "v21", "v22", "v23", "v24", "v25", "v26", "v27", "v28", "v29", "v30", "v31", "v32", "v33", "v34", "v35", "v36", "v37", "v38", "v39", "v40", "v41", "v42", "v43", "v44", "v45", "v46", "v47", "v48", "v49", "v50", "v51", "v52", "v53", "v54", "v55", "v56", "v57", "v58", "v59", "v60", "v61", "v62", "v63", "v64", "v65", "v66", "v67", "v68", "v69", "v70", "v71", "v72", "v73", "v74", "v75", "v76", "v77", "v78", "v79", "v80", "v81", "v82", "v83", "v84", "v85", "v86", "v87", "v88", "v89", "v90", "v91", "v92", "v93", "v94", "v95", "v96", "v97", "v98", "v99", "v100", "v101", "v102", "v103", "v104", "v105", "v106", "v107", "v108", "v109", "v110", "v111", "v112", "v113", "v114", "v115", "v116", "v117", "v118", "v119", "v120", "v121", "v122", "v123", "v124", "v125", "v126", "v127", "v128", "v129", "v130", "v131", "v132", "v133", "v134", "v135", "v136", "v137", "v138", "v139", "v140", "v141", "v142", "v143", "s4", "s5", "s6", "s7", "s8", "s9", "s10", "s11", "s12", "s13", "s14", "s15", "s16", "s17", "s18", "s19", "s20", "s21", "s22", "s23", "s24", "s25", "s26", "s27", "s28", "s29", "s30", "s31", "s33", "s34", "s35", "s36", "s37", "s38", "s39", "s40", "s41", "s42", "s43", "s44", "s45", "s46", "s47", "s48", "s49", "s50", "s51", "s52", "s53", "s54", "s55", "s56", "s57", "s58", "s59", "s60", "s61", "s62", "s63", "s64", "s65", "s66", "s67", "s68", "s69", "vcc", "scc", "memory");
}

extern "C" void kernel_launch(void* const* d_in, const int* in_sizes, int n_in, void* d_out, int out_size, void* d_ws, size_t ws_size,
                              hipStream_t stream) {
  const unsigned F = 0x00020000u, KV0 = 3072u * 256u, KVB = 32u * 4096u * 256u - KV0;
  fused_block<<<256, 768, 0, stream>>>((const char*)d_in[1] + KV0, KVB, F, (const char*)d_in[2] + KV0, KVB, F, d_ws, 447488u, F, d_in[0], 1280u, F,
                                       d_in[3], 640u * 8192u, F, d_in[4], 4096u * 1280u, F, d_in[6], 640u * 3456u, F, d_in[7], 1728u * 1280u, F, d_out, 1280u, F);
}
```
